```python
import torch

def _softmax(x, axis=-1):
    return torch.softmax(x, dim=axis)

def _gelu(x):
    return torch.nn.functional.gelu(x, approximate='tanh')

def _silu(x):
    return torch.nn.functional.silu(x)

def _sigmoid(x):
    return torch.sigmoid(x)

def _layernorm(x, axis=-1, eps=1e-05):
    m = x.mean(dim=axis, keepdim=True)
    v = x.var(dim=axis, keepdim=True, unbiased=False)
    return (x - m) / torch.sqrt(v + eps)

def _scatter_add(tgt, idx, src, axis=0):
    return tgt.clone().index_add_(axis, idx, src)

def _segment_sum(src, seg, n):
    out = torch.zeros((n,) + src.shape[1:], dtype=src.dtype)
    return out.index_add_(0, seg, src)

def _pad(x, pw):
    flat = tuple((v for pair in reversed(pw) for v in pair))
    return torch.nn.functional.pad(x, flat)
kernel_name = 'recurrent_scan__memory_aug_recurrent_mem_tr__dense_activation_squared_relu__prompt'

def setup_inputs():

    _w = lambda a: (lambda t: t.to(torch.bfloat16) if t.is_floating_point() else t)(torch.as_tensor(a))

    def _A_setup():
        B, S, Dm = (1, 2048, 640)
        H, Dh = (32, 192)
        M, seg = (8, 32)
        seg = min(seg, S)
        S = S // seg * seg
        return {'x': _w((lambda *s: torch.randn(*s, dtype=torch.bfloat16) * 0.05)(B, S, Dm)), 'mem0': _w((lambda *s: torch.randn(*s, dtype=torch.bfloat16) * 0.05)(B, M, Dm)), 'Wq': _w((lambda *s: torch.randn(*s, dtype=torch.bfloat16) * 1.6)(Dm, H, Dh) * Dm ** (-0.5)), 'Wk': _w((lambda *s: torch.randn(*s, dtype=torch.bfloat16) * 1.6)(Dm, H, Dh) * Dm ** (-0.5)), 'Wv': _w((lambda *s: torch.randn(*s, dtype=torch.bfloat16) * 0.05)(Dm, H, Dh) * Dm ** (-0.5)), 'Wo': _w((lambda *s: torch.randn(*s, dtype=torch.bfloat16) * 0.05)(H, Dh, Dm) * (H * Dh) ** (-0.5)), 'seg_len': seg}
    _A_in = _A_setup()

    def _B_setup():
        B, S, D, F = (1, 2048, 640, 1536)
        return {'x': _w((lambda *s: torch.randn(*s, dtype=torch.bfloat16) * 0.05)(B, S, D)), 'w1': _w((lambda *s: torch.randn(*s, dtype=torch.bfloat16) * 0.05)(D, F)), 'b1': _w((lambda *s: torch.randn(*s, dtype=torch.bfloat16) * 0.4)(F)), 'w2': _w((lambda *s: torch.randn(*s, dtype=torch.bfloat16) * 0.05)(F, D)), 'b2': _w((lambda *s: torch.randn(*s, dtype=torch.bfloat16) * 1.6)(D))}
    _B_in = _B_setup()
    return {'A_x': _A_in['x'], 'A_mem0': _A_in['mem0'], 'A_Wq': _A_in['Wq'], 'A_Wk': _A_in['Wk'], 'A_Wv': _A_in['Wv'], 'A_Wo': _A_in['Wo'], 'A_seg_len': _A_in['seg_len'], 'B_x': _B_in['x'], 'B_w1': _B_in['w1'], 'B_b1': _B_in['b1'], 'B_w2': _B_in['w2'], 'B_b2': _B_in['b2']}

def reference(A_x, A_mem0, A_Wq, A_Wk, A_Wv, A_Wo, A_seg_len, B_x, B_w1, B_b1, B_w2, B_b2):

    def _A_ref(x, mem0, Wq, Wk, Wv, Wo, seg_len):
        B, S, Dm = x.shape
        H, Dh = (Wq.shape[1], Wq.shape[2])
        M = mem0.shape[1]
        scale = Dh ** (-0.5)
        n_seg = S // seg_len
        L = M + seg_len
        idx = torch.arange(L)
        mask = (idx[:, None] < M) | (idx[:, None] >= idx[None, :])
        mem = mem0
        outs = []
        for s in range(n_seg):
            xs = x[:, s * seg_len:(s + 1) * seg_len, :].clone()
            h = torch.concatenate([mem, xs], axis=1)
            q = torch.einsum('btd,dhe->bhte', h, Wq)
            k = torch.einsum('btd,dhe->bhte', h, Wk)
            v = torch.einsum('btd,dhe->bhte', h, Wv)
            scores = torch.einsum('bhqe,bhke->bhqk', q, k) * scale
            scores = torch.where(mask[None, None], scores, -1000000000.0)
            p = _softmax(scores, axis=-1)
            o = torch.einsum('bhqk,bhke->bhqe', p, v)
            o = torch.einsum('bhte,hed->btd', o, Wo)
            mem = o[:, :M, :].clone()
            outs.append(o[:, M:, :])
        return torch.concatenate(outs, axis=1)

    def _B_ref(x, w1, b1, w2, b2):
        h = torch.einsum('bsd,df->bsf', x, w1) + b1
        zero = torch.zeros_like(h)
        r = torch.where(h > zero, h, zero)
        h = r * r
        out = torch.einsum('bsf,fd->bsd', h, w2) + b2
        return out
    x = A_x
    xn = _layernorm(x, axis=-1)
    s = _A_ref(xn, A_mem0, A_Wq, A_Wk, A_Wv, A_Wo, A_seg_len)
    g = _silu(_B_ref(xn, B_w1, B_b1, B_w2, B_b2))
    return x + s * g
```

```cpp
#include <hip/hip_runtime.h>
#include <hip/hip_bf16.h>
#include <stdint.h>

#define DI __device__ __forceinline__
#define LAS __attribute__((address_space(3)))
#define GAS __attribute__((address_space(1)))
typedef unsigned short u16;
typedef __bf16 bf16x2 __attribute__((ext_vector_type(2)));
typedef __bf16 bf16x4 __attribute__((ext_vector_type(4)));
typedef __bf16 bf16x8 __attribute__((ext_vector_type(8)));
typedef __bf16 bf16x16 __attribute__((ext_vector_type(16)));
typedef float f32x2 __attribute__((ext_vector_type(2)));
typedef float f32x4 __attribute__((ext_vector_type(4)));
typedef unsigned u32x2 __attribute__((ext_vector_type(2)));
typedef unsigned u32x4 __attribute__((ext_vector_type(4)));
typedef __amdgpu_buffer_rsrc_t rsrc_t;
typedef GAS unsigned gu32;
#define RLX_AGENT __ATOMIC_RELAXED, __HIP_MEMORY_SCOPE_AGENT

constexpr int T_ = 2048, D_ = 640, H_ = 32, E_ = 192, F_ = 1536, NS_ = 64;
constexpr unsigned MB = 1u << 20, KB = 1u << 10;
constexpr unsigned WS_XREC = 0;
constexpr unsigned WS_UM = 4 * MB;
constexpr unsigned WS_UX = 48 * MB;
constexpr unsigned WS_KX = 128 * MB;
constexpr unsigned WS_XN = 152 * MB;
constexpr unsigned WS_KM = 156 * MB;
constexpr unsigned WS_PX = 168 * MB;
constexpr unsigned WS_HF = 174 * MB;
constexpr unsigned WS_G = 180 * MB;
constexpr unsigned WS_CTL = 186 * MB;
constexpr unsigned WS_DBG = 187 * MB;
constexpr unsigned WS_END = 190 * MB;
constexpr unsigned MEM_BYTES = 10240, PCM_OFF = 12288, PCX_OFF = 16384;
constexpr int C_TKA = 0, C_TKB = 1, C_ABORT = 2, C_Q = 3, C_PAR = 8  , C_FK = 32  , C_CU = 288  , C_PS = 296  , C_PV = 328  ,
              C_F1 = 360  , C_CG = 368  , C_CP = 376  , C_N = 440;
constexpr unsigned SENT = 0xFFFFFFFFu;
constexpr unsigned SPIN_CAP = 1u << 18;
constexpr int LDS_BYTES = 160 * 1024;
constexpr int NCHAIN = 104;

struct Params {
  const u16 *x, *mem0, *Wq, *Wk, *Wv, *Wo, *w1, *b1, *w2, *b2;
  u16* out; char* ws;
};

DI rsrc_t mk_rsrc(const void* p, unsigned bytes) { return __builtin_amdgcn_make_buffer_rsrc((void*)p, (short)0, (int)bytes, 0x00020000); }
DI char* ws_base(char* ws) { return (char*)(((uintptr_t)ws + (2 * MB - 1)) & ~(uintptr_t)(2 * MB - 1)); }
DI unsigned rfl(unsigned v) { return __builtin_amdgcn_readfirstlane(v); }
DI unsigned xcc_id() { return (unsigned)__builtin_amdgcn_s_getreg(20 | (3 << 11)) & 0xfu; }
DI unsigned nmap(unsigned o) { return ((o >> 13) << 14) | (o & 8191u); }
DI unsigned pk2(float a, float b) { f32x2 f = {a, b}; return __builtin_bit_cast(unsigned, __builtin_convertvector(f, bf16x2)); }
DI unsigned pk2s(float a, float b) { unsigned v = pk2(a, b); return v < 0xFFFFFFFEu ? v : 0xFFFFFFFEu; }
DI float b2f(unsigned v) { return __uint_as_float(v << 16); }
DI bf16x8 pack8(f32x4 a, f32x4 b) { u32x4 r = {pk2(a[0], a[1]), pk2(a[2], a[3]), pk2(b[0], b[1]), pk2(b[2], b[3])}; return __builtin_bit_cast(bf16x8, r); }
#define WAIT_V(n) asm volatile("s_waitcnt vmcnt(" #n ")" ::: "memory")
#define WAIT_L0() asm volatile("s_waitcnt lgkmcnt(0)" ::: "memory")
#define BAR() do { asm volatile("" ::: "memory"); __builtin_amdgcn_s_barrier(); asm volatile("" ::: "memory"); } while (0)
#define LBAR() do { WAIT_L0(); BAR(); } while (0)
#define MFMA(a, b, c) __builtin_amdgcn_mfma_f32_16x16x32_bf16((a), (b), (c), 0, 0, 0)
#define SMF(a, b, c, i) __builtin_amdgcn_smfmac_f32_16x16x64_bf16((a), (b), (c), (i), 0, 0)

DI void glds(rsrc_t r, unsigned voff, unsigned soff, unsigned ldsdst) {
  asm volatile("s_mov_b32 m0, %0\n\ts_nop 0\n\tbuffer_load_dwordx4 %1, %2, %3 offen lds" ::"s"(ldsdst), "v"(voff), "s"(r), "s"(soff) : "memory");
}
DI bf16x4 trd(unsigned a) { return __builtin_amdgcn_ds_read_tr16_b64_v4bf16((LAS bf16x4*)a); }
DI bf16x8 cat4(bf16x4 a, bf16x4 b) { return __builtin_shufflevector(a, b, 0, 1, 2, 3, 4, 5, 6, 7); }
DI bf16x16 cat8(bf16x8 a, bf16x8 b) { return __builtin_shufflevector(a, b, 0, 1, 2, 3, 4, 5, 6, 7, 8, 9, 10, 11, 12, 13, 14, 15); }
DI bf16x8 ldsr16(unsigned a) { return *(LAS bf16x8*)a; }
DI u32x4 ld16c(rsrc_t r, unsigned v, unsigned s) { return __builtin_amdgcn_raw_buffer_load_b128(r, v, s, 16); }
DI u32x2 ld8c(rsrc_t r, unsigned v, unsigned s) { return __builtin_amdgcn_raw_buffer_load_b64(r, v, s, 16); }
DI unsigned ld4c(rsrc_t r, unsigned v, unsigned s) { return __builtin_amdgcn_raw_buffer_load_b32(r, v, s, 16); }
DI void st16c(u32x4 d, rsrc_t r, unsigned v, unsigned s) { __builtin_amdgcn_raw_buffer_store_b128(d, r, v, s, 16); asm volatile("s_nop 1" ::"v"(d) : "memory"); }
DI void st8c(u32x2 d, rsrc_t r, unsigned v, unsigned s) { __builtin_amdgcn_raw_buffer_store_b64(d, r, v, s, 16); }
DI void st4c(unsigned d, rsrc_t r, unsigned v, unsigned s) { __builtin_amdgcn_raw_buffer_store_b32(d, r, v, s, 16); }
DI unsigned umax3(unsigned a, unsigned b, unsigned c) { return max(max(a, b), c); }
DI unsigned umax4(unsigned m, u32x4 v) { return umax3(umax3(m, v[0], v[1]), v[2], v[3]); }
DI float swp32_sum(float a, float b) {
  u32x2 r = __builtin_amdgcn_permlane32_swap(__float_as_uint(a), __float_as_uint(b), false, false);
  return __uint_as_float(r[0]) + __uint_as_float(r[1]);
}
DI float x16max(float v) { u32x2 r = __builtin_amdgcn_permlane16_swap(__float_as_uint(v), __float_as_uint(v), false, false); return fmaxf(__uint_as_float(r[0]), __uint_as_float(r[1])); }
DI float x32max(float v) { u32x2 r = __builtin_amdgcn_permlane32_swap(__float_as_uint(v), __float_as_uint(v), false, false); return fmaxf(__uint_as_float(r[0]), __uint_as_float(r[1])); }
DI float x16sum(float v) { u32x2 r = __builtin_amdgcn_permlane16_swap(__float_as_uint(v), __float_as_uint(v), false, false); return __uint_as_float(r[0]) + __uint_as_float(r[1]); }
DI float x32sum(float v) { u32x2 r = __builtin_amdgcn_permlane32_swap(__float_as_uint(v), __float_as_uint(v), false, false); return __uint_as_float(r[0]) + __uint_as_float(r[1]); }
DI unsigned dpp_shr8(unsigned keep, unsigned v) { return __builtin_amdgcn_update_dpp(keep, v, 0x118, 0xf, 0xf, false); }
DI unsigned dpp_x1(unsigned v) { return __builtin_amdgcn_update_dpp(v, v, 0xB1, 0xf, 0xf, false); }

struct Ctx {
  rsrc_t ws; gu32* ctl; unsigned lds; int lane, wave; unsigned par;
};
DI gu32* cw(const Ctx& c, int i) { return c.ctl + i * 32; }
DI bool aborted(const Ctx& c) { return rfl(__hip_atomic_load(cw(c, C_ABORT), RLX_AGENT)) != 0; }
DI void do_abort(const Ctx& c, unsigned code) { if (c.lane == 0) __hip_atomic_store(cw(c, C_ABORT), code, RLX_AGENT); }
DI bool spin_fail(const Ctx& c, unsigned& spins, unsigned code) {
  ++spins;
  if (spins > SPIN_CAP) { do_abort(c, code); return true; }
  return (spins & 255u) == 0u && aborted(c);
}
DI void wait_ge(const Ctx& c, int i, unsigned want, unsigned code) {
  for (unsigned spins = 0;;) {
    if (rfl(__hip_atomic_load(cw(c, i), RLX_AGENT)) >= want) break;
    __builtin_amdgcn_s_sleep(4);
    if (spin_fail(c, spins, code)) break;
  }
  asm volatile("" ::: "memory");
}
DI unsigned xrec_soff(const Ctx& c, int s) { return WS_XREC + (unsigned)s * 65536u + (((c.par >> (s >> 5)) & 1u) << 13); }
DI unsigned um_soff(const Ctx& c, int s) { int pg = s / 3; return WS_UM + (unsigned)pg * (2 * MB) + (unsigned)(s - pg * 3) * (640 * KB) + (((c.par >> (2 + pg)) & 1u) << 13); }

template <bool PI> DI void pfmt_voff(unsigned (&v)[3], int wave, int lane, unsigned pitch) {
#pragma unroll
  for (int i = 0; i < 3; ++i) {
    unsigned n = (wave * 3 + i) * 64 + lane, row = n / 24, pos = n % 24;
    unsigned key = PI ? ((row >> 1) & 3) : (((row >> 1) & 1) | (((row >> 3) & 1) << 1));
    v[i] = row * pitch + ((((pos >> 1) ^ key) << 1) | (pos & 1)) * 16;
  }
}
DI void ufmt_voff(unsigned (&v)[3], int wave, int lane) {
#pragma unroll
  for (int i = 0; i < 3; ++i) {
    unsigned n = (wave * 3 + i) * 64 + lane, row = n >> 3, pos = n & 7;
    v[i] = row * 1280u + ((((pos >> 1) ^ ((row >> 1) & 3)) << 1) | (pos & 1)) * 16;
  }
}
DI void stage_load(rsrc_t r, const unsigned (&v)[3], unsigned soff, unsigned slot, int wave) {
#pragma unroll
  for (int i = 0; i < 3; ++i) glds(r, v[i], soff, slot + (unsigned)(wave * 3 + i) * 1024u);
}
DI unsigned lane_kx(int lane) { return (((lane >> 4) & 1) << 1) | ((lane >> 3) & 1); }
template <bool PI> DI unsigned pfmt_base(int lane, int T) { int g = lane >> 4, q = (lane >> 2) & 3; return (unsigned)(((PI ? 4 : 8) * g + q) * 384 + ((T ^ lane_kx(lane)) * 32) + (lane & 3) * 8); }
DI unsigned ufmt_base(int lane, int C) { int g = lane >> 4, q = (lane >> 2) & 3; return (unsigned)((4 * g + q) * 128 + ((C ^ lane_kx(lane)) * 32) + (lane & 3) * 8); }
template <bool PI> DI bf16x8 pfmt_frag(unsigned a, int ks) { return cat4(trd(a + ks * 32 * 384), trd(a + ks * 32 * 384 + (PI ? 16 : 4) * 384)); }
DI bf16x8 ufmt_frag(unsigned a, int ks) { return cat4(trd(a + ks * 32 * 128), trd(a + ks * 32 * 128 + 16 * 128)); }
DI bf16x16 pfmt_frag64(unsigned a) { return cat8(pfmt_frag<false>(a, 0), pfmt_frag<false>(a, 1)); }
DI bf16x16 ufmt_frag64(unsigned a, int kk) { return cat8(ufmt_frag(a, 2 * kk), ufmt_frag(a, 2 * kk + 1)); }

constexpr unsigned L_MEM = 120 * KB, L_Q = 130 * KB, L_K = 136 * KB, L_KX = 142 * KB, L_MISC = 154 * KB;
DI constexpr unsigned slot_off(int i) { return i == 0 ? 0u : i == 1 ? 72 * KB : i == 2 ? 24 * KB : i == 3 ? 120 * KB : i == 4 ? 48 * KB : 96 * KB; }
#define OPQ(x) asm volatile("" : "+v"(x))
#define SB() __builtin_amdgcn_sched_barrier(0)

#define STAMP(c, role, s, k) do {} while (0)
#define STAMP_DECL do {} while (0)
#define STAMP_FLUSH(cond, s, mask) do {} while (0)
#define STAMP_DUMP(c, role) do {} while (0)
constexpr unsigned L_STAMP = 156 * KB;

#define POLL(c, bud, code, ...) do { bool ok; \
    _Pragma("nounroll") do { asm volatile("" ::: "memory"); __VA_ARGS__; } while (!ok && --(bud) > 0); \
    if (__builtin_expect(!ok, 0)) do_abort(c, code); } while (0)
DI void poll_mem(const Ctx& c, int& bud, int s, unsigned l16, int u, unsigned la) {
  const unsigned so = xrec_soff(c, s) + u * 1024;
  la += u * 1024;
  if (u < 2) {
    POLL(c, bud, 0x100 + s, { u32x4 a = ld16c(c.ws, l16, so); u32x4 b = ld16c(c.ws, l16, so + 4096); u32x4 d = ld16c(c.ws, l16, so + 16384);
      *(LAS u32x4*)(la) = a; *(LAS u32x4*)(la + 4096) = b; *(LAS u32x4*)(la + 8192) = d;
      ok = __all(umax4(umax4(umax4(0u, a), b), d) != SENT); });
  } else {
    POLL(c, bud, 0x100 + s, { u32x4 a = ld16c(c.ws, l16, so); u32x4 b = ld16c(c.ws, l16, so + 4096);
      *(LAS u32x4*)(la) = a; *(LAS u32x4*)(la + 4096) = b;
      ok = __all(umax4(umax4(0u, a), b) != SENT); });
  }
}
DI bf16x16 ldsr32(unsigned a) { return cat8(ldsr16(a), ldsr16(a + 1024)); }
DI void ldsw32(unsigned a, bf16x16 v) {
  *(LAS bf16x8*)a = __builtin_shufflevector(v, v, 0, 1, 2, 3, 4, 5, 6, 7); *(LAS bf16x8*)(a + 1024) = __builtin_shufflevector(v, v, 8, 9, 10, 11, 12, 13, 14, 15);
}
DI float ror8(float v) { return __uint_as_float(__builtin_amdgcn_update_dpp(0u, __float_as_uint(v), 0x128, 0xf, 0xf, true)); }
DI f32x2 fold8(f32x4 v, bool hi) {
  const float o0 = hi ? v[2] : v[0], o1 = hi ? v[3] : v[1], t0 = hi ? v[0] : v[2], t1 = hi ? v[1] : v[3];
  return f32x2{o0 + ror8(t0), o1 + ror8(t1)};
}

template <int KIND> DI void s_wave(const Params& p, const Ctx& c, int h) {
  constexpr bool QW = KIND != 2;
  constexpr int NR = QW ? 7 : 8;
  constexpr unsigned LB = QW ? 0 : 72 * KB;
  const int lane = c.lane, w = c.wave, u = w & 3;
  bf16x16 W[NR][3];
  unsigned wl = LB + u * 6144 + lane * 16; OPQ(wl);
  {
    const unsigned tl = pfmt_base<false>(lane, u);
    rsrc_t rq = mk_rsrc(p.Wq, D_ * H_ * E_ * 2), rk = mk_rsrc(p.Wk, D_ * H_ * E_ * 2);
    unsigned v[3]; pfmt_voff<false>(v, w, lane, 12288);
    const unsigned s0 = h * 384;
#pragma unroll
    for (int i = 0; i < 2; ++i) { stage_load(rq, v, s0 + i * 64 * 12288, slot_off(2 * i), w); stage_load(rk, v, s0 + i * 64 * 12288, slot_off(2 * i + 1), w); }
#pragma unroll
    for (int i = 0; i < 10; ++i) {
      if (i < 9) WAIT_V(6); else WAIT_V(0);
      LBAR();
      if (i + 2 < 10) { stage_load(rq, v, s0 + (i + 2) * 64 * 12288, slot_off(2 * ((i + 2) % 3)), w); stage_load(rk, v, s0 + (i + 2) * 64 * 12288, slot_off(2 * ((i + 2) % 3) + 1), w); }
      const unsigned so = slot_off(2 * (i % 3) + (QW ? 0 : 1));
      bf16x16 f[3];
#pragma unroll
      for (int t = 0; t < 3; ++t) f[t] = pfmt_frag64(tl + t * 128 + so);
      if (i >= 7) LBAR();
#pragma unroll
      for (int t = 0; t < 3; ++t) { if (i < NR) W[i < NR ? i : 0][t] = f[t]; else ldsw32(wl + so - LB + t * 2048, f[t]); }
    }
    LBAR();
  }
  const int idx = (lane & 8) ? 0xEEEE : 0x4444;
  int bud = 1 << 20; STAMP_DECL;
  const int g = lane >> 4, r16 = lane & 15;
  unsigned l16 = lane * 16; OPQ(l16);
  unsigned memr = L_MEM + lane * 16; OPQ(memr);
  unsigned qkw = (QW ? L_Q : L_K) + (16 * u + r16) * 32 + ((g ^ (r16 >> 2)) * 8); OPQ(qkw);
  unsigned qkr = L_Q + (4 * g + (r16 >> 2)) * 32 + (((lane & 3) ^ g) * 8);
  unsigned kxa = L_KX + ((r16 >> 2) & 1) * 6144 + (g * 16 + 4 * (r16 >> 3) + (r16 & 3)) * 16;
  unsigned pxo = nmap(PCX_OFF + h * 512) + r16 * 16 + g * 4, pmo = nmap(PCM_OFF + h * 128) + r16 * 8 + (g & 1) * 4;
  if (KIND < 2) OPQ(qkr);
  if (KIND == 0) { OPQ(kxa); OPQ(pxo); OPQ(pmo); }
  const unsigned kxw = memr + (L_KX - L_MEM) + (u & 1) * 6144;
  if (KIND == 3) {
    wait_ge(c, C_FK + h, 1, 0x200);
#pragma unroll
    for (int i = 0; i < 6; ++i) *(LAS u32x4*)(kxw + i * 1024) = ld16c(c.ws, l16, WS_KX + (h * 12 + (u & 1) * 6 + i) * 1024);
  }
  const bool hi8 = lane & 8;
  const float sc = 0.07216878364870322f * 1.4426950408889634f;
  for (int s = 0; s < NS_; ++s) {
    bf16x16 bt[3];
#pragma unroll
    for (int t = 0; t < 3; ++t) bt[t] = ldsr32(wl + slot_off(2 * (NR % 3) + (QW ? 0 : 1)) - LB + t * 2048);
    f32x4 acc[3] = {};
    OPQ(acc[0]); OPQ(acc[1]); OPQ(acc[2]);
    SB();
    if (!QW) poll_mem(c, bud, s, l16, u, memr);
    if (KIND == 1) WAIT_V(0);
    LBAR();
    {
      constexpr int ord[10] = {NR, 0, 1, 2, QW ? 8 : 3, QW ? 3 : 4, QW ? 4 : 5, QW ? 5 : 6, QW ? 6 : 7, 9};
      constexpr int AWIN = QW ? 4 : 3;
      bf16x8 a[10];
#pragma unroll
      for (int i = 0; i < AWIN; ++i) { a[i] = ldsr16(memr + ord[i] * 1024); SB(); }
      STAMP(c, h, s, 0);
#pragma unroll
      for (int i = 0; i < 10; ++i) {
        const int ks = ord[i];
#pragma unroll
        for (int t = 0; t < 3; ++t) {
          acc[t] = SMF(a[i], ks < NR ? W[ks < NR ? ks : 0][t] : bt[t], acc[t], idx);
          OPQ(acc[t]);
          SB();
          if (ks >= NR && ks < 9) { bt[t] = ldsr32(wl + slot_off(2 * ((ks + 1) % 3) + (QW ? 0 : 1)) - LB + t * 2048); SB(); }
        }
        if (i + AWIN < 10) { a[i + AWIN] = ldsr16(memr + ord[i + AWIN] * 1024); SB(); }
      }
    }
#pragma unroll
    for (int t = 0; t < 3; ++t) { u32x2 v = {pk2(acc[t][0], acc[t][1]), pk2(acc[t][2], acc[t][3])}; *(LAS u32x2*)(qkw + t * 2048) = v; }
    bf16x8 kx[2][6];
    u32x4 kxn[6];
    if (KIND == 0) {
      SB();
#pragma unroll
      for (int m = 0; m < 6; ++m) { kx[0][m] = ldsr16(kxa + m * 1024); kx[1][m] = ldsr16(kxa + m * 1024 + 128); }
      SB();
    }
    if (KIND == 1) { if (lane == 0 && s > 0) __hip_atomic_store(cw(c, C_PS + h), (unsigned)s, RLX_AGENT); }
    if (KIND == 3) {
      const int sn = s + 1 < NS_ ? s + 1 : s;
      if ((sn & 7) == 0) wait_ge(c, C_FK + (sn >> 3) * 32 + h, 1, 0x201);
#pragma unroll
      for (int i = 0; i < 6; ++i) kxn[i] = ld16c(c.ws, l16, WS_KX + ((sn * 32 + h) * 12 + (u & 1) * 6 + i) * 1024);
    }
    LBAR();
    if (KIND == 0) {
      if (s + 1 < NS_) {
        STAMP(c, h, s, 1);
        bf16x8 qf[6], kf[6];
        f32x4 s0 = {}, s1 = {}, sm = {};
#pragma unroll
        for (int m = 0; m < 2; ++m) { qf[m] = cat4(trd(qkr + m * 1024), trd(qkr + m * 1024 + 512)); SB(); }
#pragma unroll
        for (int m = 0; m < 6; ++m) {
          if (m + 2 < 6) { qf[m + 2] = cat4(trd(qkr + (m + 2) * 1024), trd(qkr + (m + 2) * 1024 + 512)); SB(); }
          s0 = MFMA(kx[0][m], qf[m], s0); s1 = MFMA(kx[1][m], qf[m], s1);
          OPQ(s0); OPQ(s1); SB();
          kf[m] = cat4(trd(qkr + (L_K - L_Q) + m * 1024), trd(qkr + (L_K - L_Q) + m * 1024 + 512)); SB();
        }
#pragma unroll
        for (int m = 0; m < 3; ++m) sm = MFMA(kf[m], qf[m], sm);
        OPQ(sm); SB();
        f32x2 y0 = fold8(s0, hi8) * sc; OPQ(y0); SB();
        sm = MFMA(kf[3], qf[3], sm); OPQ(sm); SB();
        f32x2 y1 = fold8(s1, hi8) * sc; OPQ(y1); SB();
        sm = MFMA(kf[4], qf[4], sm); OPQ(sm); SB();
        f32x2 e0 = {__builtin_amdgcn_exp2f(y0[0]), __builtin_amdgcn_exp2f(y0[1])}; OPQ(e0); SB();
        sm = MFMA(kf[5], qf[5], sm); OPQ(sm); SB();
        f32x2 e1 = {__builtin_amdgcn_exp2f(y1[0]), __builtin_amdgcn_exp2f(y1[1])};
        f32x2 ls = e0 + e1; float lx = ls[0] + ls[1]; OPQ(lx); SB();
        f32x2 ym = fold8(sm, hi8);
        float z = swp32_sum(ym[0], ym[1]) * sc;
        float em = __builtin_amdgcn_exp2f(z);
        float l = lx + em; l += ror8(l); l = x32sum(x16sum(l));
        if (__builtin_expect(!__all(__float_as_uint(l) - 0x21800000u < 0x50000000u), 0)) {
          float mx = fmaxf(fmaxf(fmaxf(y0[0], y0[1]), fmaxf(y1[0], y1[1])), z);
          mx = fmaxf(mx, ror8(mx)); mx = x32max(x16max(mx));
          e0[0] = __builtin_amdgcn_exp2f(y0[0] - mx); e0[1] = __builtin_amdgcn_exp2f(y0[1] - mx);
          e1[0] = __builtin_amdgcn_exp2f(y1[0] - mx); e1[1] = __builtin_amdgcn_exp2f(y1[1] - mx); em = __builtin_amdgcn_exp2f(z - mx);
          l = (e0[0] + e0[1]) + (e1[0] + e1[1]) + em; l += ror8(l); l = x32sum(x16sum(l));
          if (!(l == l)) { l = 1.0f; e0[0] = e0[1] = e1[0] = e1[1] = em = __uint_as_float(0x7FC00000u); }
        }
        const float inv = __builtin_amdgcn_rcpf(l);
        const unsigned so = xrec_soff(c, s);
        e0 *= inv; e1 *= inv; em *= inv;
        st4c(pk2(e0[0], e0[1]), c.ws, pxo, so);
        st4c(pk2(e1[0], e1[1]), c.ws, pxo + 256, so);
        u32x2 r = __builtin_amdgcn_permlane32_swap(__float_as_uint(em), __float_as_uint(em), false, false);
        st4c(pk2(__uint_as_float(r[0]), __uint_as_float(r[1])), c.ws, pmo, so);
        STAMP(c, h, s, 2);
      }
    }
    if (KIND == 1) {
#pragma unroll
      for (int m = 0; m < 6; ++m) st16c(__builtin_bit_cast(u32x4, cat4(trd(qkr + (L_K - L_Q) + m * 1024), trd(qkr + (L_K - L_Q) + m * 1024 + 512))), c.ws, l16, WS_KM + ((s * 32 + h) * 6 + m) * 1024);
    }
    if (KIND == 3) {
#pragma unroll
      for (int i = 0; i < 6; ++i) *(LAS u32x4*)(kxw + i * 1024) = kxn[i];
    }
    STAMP_FLUSH(w == 0, s, 7);
  }
  if (KIND == 1) { WAIT_V(0); if (lane == 0) __hip_atomic_store(cw(c, C_PS + h), (unsigned)NS_, RLX_AGENT); }
}

template <bool AW> DI void v_wave(const Params& p, const Ctx& c, int h) {
  constexpr int N1 = AW ? 2 : 1, N2R = AW ? 2 : 5;
  const int lane = c.lane, w = c.wave, u = w & 3;
  bf16x16 W1[8][N1], W2[N2R][3];
  unsigned w1l = 72 * KB + (AW ? u * 4096 : 16384 + u * 2048) + lane * 16; OPQ(w1l);
  unsigned w2l = u * 6144 + lane * 16;
  if (AW) OPQ(w2l);
  {
    const unsigned tl = pfmt_base<false>(lane, AW ? u : u + 8), ul = ufmt_base(lane, u);
    rsrc_t rv = mk_rsrc(p.Wv, D_ * H_ * E_ * 2), ro = mk_rsrc(p.Wo, D_ * H_ * E_ * 2);
    unsigned v[3], vu[3]; pfmt_voff<false>(v, w, lane, 12288); ufmt_voff(vu, w, lane);
    const unsigned s0 = h * 384, u0 = h * 192 * 1280;
#pragma unroll
    for (int i = 0; i < 2; ++i) { stage_load(ro, vu, u0 + i * 128, slot_off(2 * i), w); stage_load(rv, v, s0 + i * 64 * 12288, slot_off(2 * i + 1), w); }
#pragma unroll
    for (int i = 0; i < 10; ++i) {
      if (i < 9) WAIT_V(6); else WAIT_V(0);
      LBAR();
      if (i + 2 < 10) { stage_load(ro, vu, u0 + (i + 2) * 128, slot_off(2 * ((i + 2) % 3)), w); stage_load(rv, v, s0 + (i + 2) * 64 * 12288, slot_off(2 * ((i + 2) % 3) + 1), w); }
      bf16x16 f1[N1], f2[3];
      const bool mine = AW ? i >= 5 : i < 5;
#pragma unroll
      for (int t = 0; t < N1; ++t) f1[t] = pfmt_frag64(tl + t * 128 + slot_off(2 * (i % 3) + 1));
      if (mine) {
#pragma unroll
        for (int kk = 0; kk < 3; ++kk) f2[kk] = ufmt_frag64(ul + slot_off(2 * (i % 3)), kk);
      }
      if (i >= 7) LBAR();
#pragma unroll
      for (int t = 0; t < N1; ++t) { if (i < 8) W1[i < 8 ? i : 0][t] = f1[t]; else ldsw32(w1l + slot_off(2 * (i % 3) + 1) - 72 * KB + t * 2048, f1[t]); }
      if (mine) {
#pragma unroll
        for (int kk = 0; kk < 3; ++kk) { if (i < 7) W2[AW ? (i == 5 ? 0 : 1) : (i < 5 ? i : 0)][kk] = f2[kk]; else ldsw32(w2l + slot_off(2 * (i % 3)) + kk * 2048, f2[kk]); }
      }
    }
    LBAR();
  }
  const int idx = (lane & 8) ? 0xEEEE : 0x4444;
  int bud = 1 << 20; STAMP_DECL;
  const int g = lane >> 4, r16 = lane & 15;
  unsigned l16 = lane * 16; OPQ(l16);
  unsigned memr = L_MEM + lane * 16; OPQ(memr);
  const int ra = 4 * (g & 1) + 2 * (g >> 1);
  unsigned vw = L_Q + (((r16 >> 3) * 16) + ((r16 >> 1) & 1) * 8 + ra) * 16 + ((r16 >> 2) & 1) * 8 + (r16 & 1) * 2 + (u >> 1) * 512 + (u & 1) * 4 + (AW ? 0 : 2048); OPQ(vw);
  unsigned stv = h * 256 + r16 * 16 + (g & 1) * 8 + u * 16384 + (lane >> 5) * 65536 + (AW ? 5 * 65536 : 0); OPQ(stv);
  auto put = [&](f32x4 a, f32x4 b, unsigned so, bool pair) {
    u32x2 d = {pk2s(swp32_sum(a[0], b[0]), swp32_sum(a[1], b[1])), pk2s(swp32_sum(a[2], b[2]), swp32_sum(a[3], b[3]))};
    if (pair || lane < 32) st8c(d, c.ws, stv, so);
  };
  for (int s = 0; s < NS_; ++s) {
    bf16x16 bt[N1];
#pragma unroll
    for (int t = 0; t < N1; ++t) bt[t] = ldsr32(w1l + slot_off(5) - 72 * KB + t * 2048);
    f32x4 acc[N1] = {};
#pragma unroll
    for (int t = 0; t < N1; ++t) OPQ(acc[t]);
    SB();
    if (!AW) poll_mem(c, bud, s, l16, u, memr);
    WAIT_V(0);
    LBAR();
    {
      constexpr int ord[10] = {8, 0, 1, 2, 3, 4, 5, 6, 7, 9};
      constexpr int AWIN = AW ? 4 : 6;
      bf16x8 a[10];
#pragma unroll
      for (int i = 0; i < AWIN; ++i) { a[i] = ldsr16(memr + ord[i] * 1024); SB(); }
      STAMP(c, 32 + h, s, 0);
#pragma unroll
      for (int i = 0; i < 10; ++i) {
        const int ks = ord[i];
#pragma unroll
        for (int t = 0; t < N1; ++t) {
          acc[t] = SMF(a[i], ks < 8 ? W1[ks < 8 ? ks : 0][t] : bt[t], acc[t], idx);
          OPQ(acc[t]); SB();
          if (ks == 8) { bt[t] = ldsr32(w1l + slot_off(1) - 72 * KB + t * 2048); SB(); }
        }
        if (i + AWIN < 10) { a[i + AWIN] = ldsr16(memr + ord[i + AWIN] * 1024); SB(); }
      }
    }
#pragma unroll
    for (int t = 0; t < N1; ++t) {
      unsigned v = pk2(swp32_sum(acc[t][0], acc[t][2]), swp32_sum(acc[t][1], acc[t][3]));
      *(LAS u16*)(vw + t * 1024) = (u16)v; *(LAS u16*)(vw + t * 1024 + 16) = (u16)(v >> 16);
    }
    SB();
    bf16x16 ut[3];
    if (AW) {
#pragma unroll
      for (int kk = 0; kk < 3; ++kk) ut[kk] = ldsr32(w2l + slot_off(2) + kk * 2048);
    }
    if (w == 4 && lane == 0 && s > 0) __hip_atomic_store(cw(c, C_PV + h), (unsigned)s, RLX_AGENT);
    const unsigned so = um_soff(c, s);
    LBAR();
    bf16x8 va[3];
#pragma unroll
    for (int kk = 0; kk < 3; ++kk) va[kk] = ldsr16(memr + (L_Q - L_MEM) + kk * 1024);
    SB();
    auto tile = [&](const bf16x16 (&b)[3]) { f32x4 o = {};
#pragma unroll
      for (int kk = 0; kk < 3; ++kk) o = SMF(va[kk], b[kk], o, idx);
      OPQ(o); SB();
      return o; };
    auto refill = [&](unsigned off) {
#pragma unroll
      for (int kk = 0; kk < 3; ++kk) ut[kk] = ldsr32(w2l + off + kk * 2048);
      SB(); };
    if (AW) {
      f32x4 o7 = tile(ut); refill(slot_off(4));
      f32x4 o5 = tile(W2[0]), o6 = tile(W2[1]);
      f32x4 o8 = tile(ut); refill(slot_off(0));
      put(o5, o6, so, true); SB();
      f32x4 o9 = tile(ut);
      put(o7, o8, so + 2 * 65536, true); put(o9, o9, so + 4 * 65536, false);
    } else {
      f32x4 o0 = tile(W2[0]), o1 = tile(W2[1]), o2 = tile(W2[AW ? 0 : 2]), o3 = tile(W2[AW ? 0 : 3]), o4 = tile(W2[AW ? 0 : 4]);
      put(o0, o1, so, true); put(o2, o3, so + 2 * 65536, true); put(o4, o4, so + 4 * 65536, false);
    }
    STAMP(c, 32 + h, s, 2);
    if (s + 2 < NS_) {
      const unsigned sr = um_soff(c, s + 2); const u32x2 d = {SENT, SENT};
      st8c(d, c.ws, stv, sr); st8c(d, c.ws, stv, sr + 2 * 65536); if (lane < 32) st8c(d, c.ws, stv, sr + 4 * 65536);
    }
    STAMP_FLUSH(w == 0, s, 5);
  }
  WAIT_V(0);
  BAR();
  if (w == 0 && lane == 0) __hip_atomic_store(cw(c, C_PV + h), (unsigned)NS_, RLX_AGENT);
}

DI void m_wave(const Params& p, const Ctx& c, int ct) {
  const int lane = c.lane, w = c.wave;
  const int idx = (lane & 8) ? 0xEEEE : 0x4444;
  int bud = 1 << 20; STAMP_DECL;
  const int g = lane >> 4, r16 = lane & 15;
  unsigned l16 = lane * 16; OPQ(l16);
  unsigned vm = nmap(PCM_OFF) + g * 256 + r16 * 8; OPQ(vm);
  unsigned vpub = nmap(ct * 256) + ((r16 >> 1) & 1) * 128 + (4 * (g & 1) + 2 * (g >> 1) + (r16 & 1)) * 16 + (r16 >> 2) * 4; OPQ(vpub);
  volatile LAS unsigned* done = (volatile LAS unsigned*)(L_MISC);
  if (w == 0) *done = 0;
  LBAR();
  for (int s = w; s < NS_ - 1; s += 8) {
    u32x4 bx[32], bm[8];
    wait_ge(c, C_CU + (s >> 3), 32, 0x300);
#pragma unroll
    for (int i = 0; i < 32; ++i) bx[i] = ld16c(c.ws, l16, WS_UX + ((s * 40 + ct) * 32 + i) * 1024);
    SB();
    for (int sp = 0; rfl(*done) < (unsigned)s && sp < (1 << 24); ++sp) __builtin_amdgcn_s_sleep(2);
    const unsigned so = xrec_soff(c, s), su = um_soff(c, s) + ct * 16384, sn = xrec_soff(c, s + 1);
    POLL(c, bud, 0x400 + s, {
      unsigned m = 0;
      _Pragma("unroll") for (int i = 0; i < 8; ++i) bm[i] = ld16c(c.ws, l16, su + i * 1024);
      SB();
      _Pragma("unroll") for (int i = 0; i < 8; ++i) m = umax4(m, bm[i]);
      ok = __all(m != SENT); });
    STAMP(c, 64 + ct, s, 0);
    unsigned res;
    POLL(c, bud, 0x500 + s, {
      u32x4 ax[16]; u32x2 am[8];
      _Pragma("unroll") for (int i = 0; i < 16; ++i) ax[i] = ld16c(c.ws, l16, so + nmap(PCX_OFF + i * 1024));
      _Pragma("unroll") for (int i = 0; i < 8; ++i) am[i] = ld8c(c.ws, vm, so + (i >> 1) * 1024 + (i & 1) * 128);
      f32x4 acc = {};
      SB();
      _Pragma("unroll") for (int i = 0; i < 16; ++i) acc = SMF(__builtin_bit_cast(bf16x8, ax[i]), cat8(__builtin_bit_cast(bf16x8, bx[2 * i]), __builtin_bit_cast(bf16x8, bx[2 * i + 1])), acc, idx);
      _Pragma("unroll") for (int i = 0; i < 4; ++i) { u32x4 a4 = {am[2 * i][0], am[2 * i][1], am[2 * i + 1][0], am[2 * i + 1][1]};
        acc = SMF(__builtin_bit_cast(bf16x8, a4), cat8(__builtin_bit_cast(bf16x8, bm[2 * i]), __builtin_bit_cast(bf16x8, bm[2 * i + 1])), acc, idx); }
      const float s0 = swp32_sum(acc[0], acc[2]); const float s1 = swp32_sum(acc[1], acc[3]);
      const bool odd = lane & 1;
      const float t = __uint_as_float(dpp_x1(__float_as_uint(odd ? s0 : s1)));
      res = pk2s(odd ? t : s0, odd ? s1 : t);
      ok = __all(s0 == s0 && s1 == s1);
      if (!ok) { unsigned m = 0;
        _Pragma("unroll") for (int i = 0; i < 16; ++i) m = umax4(m, ax[i]);
        _Pragma("unroll") for (int i = 0; i < 8; ++i) m = umax3(m, am[i][0], am[i][1]);
        ok = __all(m != SENT); } });
    st4c(res, c.ws, vpub, sn);
    STAMP(c, 64 + ct, s, 2);
    *done = (unsigned)(s + 1);
    STAMP_FLUSH(true, s, 5);
  }
}

template <bool SC> DI void xload(u32x4& d, rsrc_t r, unsigned v, unsigned s) {
  if (SC) asm volatile("buffer_load_dwordx4 %0, %1, %2, %3 offen sc1" : "=v"(d) : "v"(v), "s"(r), "s"(s) : "memory");
  else asm volatile("buffer_load_dwordx4 %0, %1, %2, %3 offen" : "=v"(d) : "v"(v), "s"(r), "s"(s) : "memory");
}
#define XPIN(a, b) asm volatile("" : "+v"(a), "+v"(b))
constexpr int WIN = 4;
constexpr unsigned STG = 24 * KB;

struct WStream { rsrc_t r; unsigned v[3]; unsigned s0, step; };

struct Ring { unsigned rb[4], cur[4], slot; };
DI void ring_next(Ring& r) {
  r.slot = r.slot == 2 * STG ? 0u : r.slot + STG;
#pragma unroll
  for (int j = 0; j < 4; ++j) { r.cur[j] = r.rb[j] + r.slot; OPQ(r.cur[j]); }
}
template <bool PI, int NST, bool XSC, int MODE>
DI void proj_stage(const Ctx& c, f32x4 (&acc)[12][2], u32x4 (&X)[2][2], bf16x8 (&A)[WIN], Ring& rg, int t, const WStream& ws, unsigned xs, unsigned xts, const WStream& nx, bool has_next, unsigned l16) {
  auto xl = [&](int ks) { xload<XSC>(X[ks][0], c.ws, l16, xs + (2 * t + 4 + ks) * 1024); xload<XSC>(X[ks][1], c.ws, l16, xs + (xts + 2 * t + 4 + ks) * 1024); };
#pragma unroll
  for (int q = 0; q < 24; ++q) {
    const int ks = q / 12, T = q % 12;
    if (q == 0) XPIN(X[0][0], X[0][1]);
    if (q == 12) {
      if (MODE == 1) { if (has_next) WAIT_V(7); else WAIT_V(4); } else if (MODE == 2) { if (has_next) WAIT_V(3); else WAIT_V(0); } else WAIT_V(7);
      XPIN(X[1][0], X[1][1]);
      if (MODE == 0) xl(0);
    }
    const bf16x8 a = A[q % WIN];
    acc[T][0] = MFMA(a, __builtin_bit_cast(bf16x8, X[ks][0]), acc[T][0]);
    acc[T][1] = MFMA(a, __builtin_bit_cast(bf16x8, X[ks][1]), acc[T][1]);
    XPIN(acc[T][0], acc[T][1]);
    SB();
    if (q == 23 && MODE == 0) xl(1);
    if (q + WIN == 24 && MODE != 2) {
      if (MODE == 1) { if (has_next) WAIT_V(5); else WAIT_V(2); } else WAIT_V(7);
      LBAR();
      if (MODE == 0 && t + 3 < NST) stage_load(ws.r, ws.v, ws.s0 + (t + 3) * ws.step, rg.slot, c.wave);
      else if (has_next) stage_load(nx.r, nx.v, nx.s0 + (MODE == 1 ? nx.step : 0u), rg.slot, c.wave);
      ring_next(rg);
      SB();
    }
    if (q + WIN < 24 || MODE != 2) {
      const int f = (q + WIN) % 24;
      A[q % WIN] = pfmt_frag<PI>(rg.cur[(f % 12) & 3] + ((f % 12) >> 2) * 128, f / 12);
      SB();
    }
  }
}
template <bool PI, int NST, bool XSC> DI void proj(const Ctx& c, f32x4 (&acc)[12][2], const WStream& ws, unsigned xs, unsigned xts, const WStream& nx, bool has_next, unsigned l16) {
  const int w = c.wave;
  Ring rg;
#pragma unroll
  for (int j = 0; j < 4; ++j) rg.rb[j] = pfmt_base<PI>(c.lane, j);
  u32x4 X0[2][2], X1[2][2];
  stage_load(ws.r, ws.v, ws.s0, 0, w);
#pragma unroll
  for (int i = 0; i < 4; ++i) xload<XSC>(X0[i >> 1][i & 1], c.ws, l16, xs + ((i & 1) * xts + (i >> 1)) * 1024);
  stage_load(ws.r, ws.v, ws.s0 + ws.step, STG, w);
#pragma unroll
  for (int i = 0; i < 4; ++i) xload<XSC>(X1[i >> 1][i & 1], c.ws, l16, xs + ((i & 1) * xts + 2 + (i >> 1)) * 1024);
  stage_load(ws.r, ws.v, ws.s0 + 2 * ws.step, 2 * STG, w);
#pragma unroll
  for (int T = 0; T < 12; ++T) { acc[T][0] = f32x4{}; acc[T][1] = f32x4{}; }
  bf16x8 A[WIN];
  rg.slot = 2 * STG; ring_next(rg);
  WAIT_V(12);
  LBAR();
#pragma unroll
  for (int f = 0; f < WIN; ++f) A[f] = pfmt_frag<PI>(rg.cur[f & 3] + (f >> 2) * 128, 0);
  SB();
#pragma nounroll
  for (int t = 0; t < NST - 2; t += 2) {
    proj_stage<PI, NST, XSC, 0>(c, acc, X0, A, rg, t, ws, xs, xts, nx, has_next, l16);
    proj_stage<PI, NST, XSC, 0>(c, acc, X1, A, rg, t + 1, ws, xs, xts, nx, has_next, l16);
  }
  proj_stage<PI, NST, XSC, 1>(c, acc, X0, A, rg, NST - 2, ws, xs, xts, nx, has_next, l16);
  proj_stage<PI, NST, XSC, 2>(c, acc, X1, A, rg, NST - 1, ws, xs, xts, nx, has_next, l16);
}

template <bool LAST> DI void u_stage(const Ctx& c, const bf16x8 (&VF)[6][2], f32x4 (&o)[2][2], bf16x8 (&B)[WIN], Ring& rg, int t, const WStream& ws, unsigned uo, unsigned l16) {
#pragma unroll
  for (int q = 0; q < 24; ++q) {
    const int m = q % 6, C = q / 6;
    const bf16x8 b = B[q % WIN];
    o[C & 1][0] = MFMA(VF[m][0], b, m == 0 ? f32x4{} : o[C & 1][0]);
    o[C & 1][1] = MFMA(VF[m][1], b, m == 0 ? f32x4{} : o[C & 1][1]);
    XPIN(o[C & 1][0], o[C & 1][1]);
    SB();
    if (m == 1 && (C > 0 || t > 0)) { st16c(__builtin_bit_cast(u32x4, pack8(o[(C + 1) & 1][0], o[(C + 1) & 1][1])), c.ws, l16, uo + (4 * t + C - 1) * 32768); SB(); }
    if (q + WIN == 24 && !LAST) {
      WAIT_V(6);
      LBAR();
      if (t + 3 < 10) stage_load(ws.r, ws.v, ws.s0 + (t + 3) * ws.step, rg.slot, c.wave);
      ring_next(rg);
      SB();
    }
    if (q + WIN < 24 || !LAST) { const int f = (q + WIN) % 24; B[q % WIN] = ufmt_frag(rg.cur[f / 6], f % 6); SB(); }
  }
}
DI void uphase(const Ctx& c, const f32x4 (&acc)[12][2], const WStream& ws, unsigned uo, unsigned l16) {
  Ring rg;
#pragma unroll
  for (int j = 0; j < 4; ++j) rg.rb[j] = ufmt_base(c.lane, j);
  bf16x8 B[WIN];
  rg.slot = 0; ring_next(rg);
  WAIT_V(3);
  LBAR();
  stage_load(ws.r, ws.v, ws.s0 + 2 * ws.step, 0, c.wave);
#pragma unroll
  for (int f = 0; f < WIN; ++f) B[f] = ufmt_frag(rg.cur[0], f);
  SB();
  bf16x8 VF[6][2];
#pragma unroll
  for (int m = 0; m < 6; ++m) { VF[m][0] = pack8(acc[2 * m][0], acc[2 * m + 1][0]); VF[m][1] = pack8(acc[2 * m][1], acc[2 * m + 1][1]); XPIN(VF[m][0], VF[m][1]); }
  SB();
  f32x4 o[2][2] = {};
#pragma nounroll
  for (int t = 0; t < 9; ++t) u_stage<false>(c, VF, o, B, rg, t, ws, uo, l16);
  u_stage<true>(c, VF, o, B, rg, 9, ws, uo, l16);
  st16c(__builtin_bit_cast(u32x4, pack8(o[1][0], o[1][1])), c.ws, l16, uo + 39 * 32768);
}

DI void signal_wg(const Ctx& c, int word) {
  WAIT_V(0);
  BAR();
  if (c.wave == 0 && c.lane == 0) __hip_atomic_fetch_add(cw(c, word), 1u, RLX_AGENT);
}

DI void attn_tail(const Ctx& c, const f32x4 (&acc)[12][2], int s, int h, unsigned l16) {
  const int lane = c.lane, g = lane >> 4, tok = lane & 15;
  bf16x8 QF[6][2];
#pragma unroll
  for (int m = 0; m < 6; ++m) { QF[m][0] = pack8(acc[2 * m][0], acc[2 * m + 1][0]); QF[m][1] = pack8(acc[2 * m][1], acc[2 * m + 1][1]); }
  wait_ge(c, C_FK + (s >> 3) * 32 + h, 1, 0x600);
  f32x4 x00 = {}, x10 = {}, x11 = {}, m0 = {}, m1 = {};
  {
    u32x4 kx[12];
#pragma unroll
    for (int i = 0; i < 12; ++i) kx[i] = ld16c(c.ws, l16, WS_KX + ((s * 32 + h) * 12 + i) * 1024);
    SB();
#pragma unroll
    for (int m = 0; m < 6; ++m) {
      x00 = MFMA(__builtin_bit_cast(bf16x8, kx[m]), QF[m][0], x00);
      x10 = MFMA(__builtin_bit_cast(bf16x8, kx[m]), QF[m][1], x10);
      x11 = MFMA(__builtin_bit_cast(bf16x8, kx[6 + m]), QF[m][1], x11);
    }
  }
  wait_ge(c, C_PS + h, s + 1, 0x601);
  {
    u32x4 km[6];
#pragma unroll
    for (int i = 0; i < 6; ++i) km[i] = ld16c(c.ws, l16, WS_KM + ((s * 32 + h) * 6 + i) * 1024);
    SB();
#pragma unroll
    for (int m = 0; m < 6; ++m) { m0 = MFMA(__builtin_bit_cast(bf16x8, km[m]), QF[m][0], m0); m1 = MFMA(__builtin_bit_cast(bf16x8, km[m]), QF[m][1], m1); }
  }
  const float sc = 0.07216878364870322f * 1.4426950408889634f, NEG = -1.0e9f * 1.4426950408889634f;
#pragma unroll
  for (int a = 0; a < 2; ++a) {
    f32x4 lo = a ? x10 : x00, hi = x11, mm = a ? m1 : m0;
#pragma unroll
    for (int i = 0; i < 4; ++i) {
      mm[i] = x32sum(mm[i]) * sc;
      const bool keep = 4 * g + i <= tok;
      if (a == 0) { lo[i] = keep ? lo[i] * sc : NEG; hi[i] = NEG; } else { lo[i] *= sc; hi[i] = keep ? hi[i] * sc : NEG; }
    }
    float mx = fmaxf(fmaxf(fmaxf(lo[0], lo[1]), fmaxf(lo[2], lo[3])), fmaxf(fmaxf(mm[0], mm[1]), fmaxf(mm[2], mm[3])));
    mx = fmaxf(mx, fmaxf(fmaxf(hi[0], hi[1]), fmaxf(hi[2], hi[3])));
    mx = x32max(x16max(mx));
    float l = 0.f, lm = 0.f;
#pragma unroll
    for (int i = 0; i < 4; ++i) {
      lo[i] = __builtin_amdgcn_exp2f(lo[i] - mx); hi[i] = __builtin_amdgcn_exp2f(hi[i] - mx); mm[i] = __builtin_amdgcn_exp2f(mm[i] - mx);
      l += lo[i] + hi[i]; lm += mm[i];
    }
    l = x32sum(x16sum(l + 0.5f * lm));
    const float inv = 1.0f / l;
    const unsigned po = WS_PX + (s * 2 + a) * 40960;
    st16c(__builtin_bit_cast(u32x4, pack8(lo * inv, hi * inv)), c.ws, l16, po + h * 1024);
    u32x2 pm = {pk2(mm[0] * inv, mm[1] * inv), pk2(mm[2] * inv, mm[3] * inv)};
    if (g < 2) st8c(pm, c.ws, tok * 16 + g * 8, po + 32768 + h * 256);
  }
  WAIT_V(0);
  if (lane == 0) __hip_atomic_fetch_add(cw(c, C_CP + s), 1u, RLX_AGENT);
}

DI void fin_item(const Params& p, const Ctx& c, int s, int part, unsigned l16) {
  const int lane = c.lane, w = c.wave, g = lane >> 4, tok = lane & 15;
  if (w == 0) {
    wait_ge(c, C_CP + s, 32, 0x700); wait_ge(c, C_CU + (s >> 3), 32, 0x701); wait_ge(c, C_CG + (s >> 3), 4, 0x702);
    for (unsigned spins = 0;;) {
      if (__all(__hip_atomic_load(cw(c, C_PV + (lane & 31)), RLX_AGENT) > (unsigned)s)) break;
      __builtin_amdgcn_s_sleep(4);
      if (spin_fail(c, spins, 0x703)) break;
    }
  }
  LBAR();
  const int ct = part * 8 + w;
  u32x4 u[20];
  {
    u32x4 pv[10];
#pragma unroll
    for (int i = 0; i < 10; ++i) pv[i] = ld16c(c.ws, l16, WS_PX + s * 81920 + (w * 10 + i) * 1024);
#pragma unroll
    for (int i = 0; i < 20; ++i) u[i] = ld16c(c.ws, l16, WS_UX + ((s * 40 + ct) * 32 + i) * 1024);
    SB();
#pragma unroll
    for (int i = 0; i < 10; ++i) *(LAS u32x4*)(l16 + (w * 10 + i) * 1024) = pv[i];
  }
  u32x4 u2[20];
  const unsigned su = um_soff(c, s) + ct * 16384;
#pragma unroll
  for (int i = 0; i < 20; ++i) u2[i] = i < 12 ? ld16c(c.ws, l16, WS_UX + ((s * 40 + ct) * 32 + 20 + i) * 1024) : ld16c(c.ws, l16, su + (i - 12) * 1024);
  f32x4 gt[2]; u32x2 xv[2];
#pragma unroll
  for (int a = 0; a < 2; ++a) {
    gt[a] = __builtin_bit_cast(f32x4, ld16c(c.ws, l16, WS_G + ((s * 2 + a) * 40 + ct) * 1024));
    xv[a] = *(const u32x2*)(p.x + (size_t)(s * 32 + a * 16 + tok) * D_ + ct * 16 + g * 4);
  }
  SB();
  LBAR();
  unsigned pa = l16, pb = l16 + 40960; OPQ(pa); OPQ(pb);
  f32x4 o0 = {}, o1 = {};
#pragma unroll
  for (int i = 0; i < 40; ++i) {
    const bf16x8 a = __builtin_bit_cast(bf16x8, i < 20 ? u[i < 20 ? i : 0] : u2[i < 20 ? 0 : i - 20]);
    o0 = MFMA(a, ldsr16(pa + i * 1024), o0); o1 = MFMA(a, ldsr16(pb + i * 1024), o1);
  }
#pragma unroll
  for (int a = 0; a < 2; ++a) {
    const f32x4 o = a ? o1 : o0;
    u32x2 r = {pk2(b2f(xv[a][0] & 0xffffu) + o[0] * gt[a][0], __uint_as_float(xv[a][0] & 0xffff0000u) + o[1] * gt[a][1]),
               pk2(b2f(xv[a][1] & 0xffffu) + o[2] * gt[a][2], __uint_as_float(xv[a][1] & 0xffff0000u) + o[3] * gt[a][3])};
    *(u32x2*)(p.out + (size_t)(s * 32 + a * 16 + tok) * D_ + ct * 16 + g * 4) = r;
  }
}

constexpr int N_A = 512, N_F1 = 64, N_F2 = 32, N_QF = 256 + 320, N_ITEMS = N_A + N_F1 + N_F2 + N_QF;
DI int next_item(const Ctx& c) {
  LAS unsigned* tk = (LAS unsigned*)(LDS_BYTES - 16);
  LBAR();
  if (c.wave == 0 && c.lane == 0) *tk = __hip_atomic_fetch_add(cw(c, C_Q), 1u, RLX_AGENT);
  LBAR();
  return (int)rfl(*tk);
}
DI void bulk(const Params& p, const Ctx& c, int lo, int hi) {
  const int lane = c.lane, w = c.wave, g = lane >> 4;
  unsigned l16 = lane * 16; OPQ(l16);
#define BSTAMP(k, v) do {} while (0)
  for (;;) {
    const int it = lo + next_item(c);
    if (it >= hi) break;
    int type, rb, h;
    if (it < N_A) { rb = it >> 6; h = it & 31; type = (it >> 5) & 1; }
    else if (it < N_A + N_F1) { type = 2; rb = (it - N_A) >> 3; h = (it - N_A) & 7; }
    else if (it < N_A + N_F1 + N_F2) { type = 4; rb = (it - N_A - N_F1) >> 2; h = (it - N_A - N_F1) & 3; }
    else {
      int j = it - (N_A + N_F1 + N_F2);
      if (j < 32) { type = 3; rb = 0; h = j; }
      else { j -= 32; const int b = j / 72, r = j - b * 72; if (b < 7 && r < 32) { type = 3; rb = b + 1; h = r; } else { type = 5; rb = b; h = b < 7 ? r - 32 : r; } }
    }
    const int s = rb * 8 + w;
    if (type == 5) { fin_item(p, c, rb * 8 + h / 5, h % 5, l16); continue; }
    f32x4 acc[12][2];
    if (type == 4) {
      if (w == 0) wait_ge(c, C_F1 + rb, 8, 0x800);
      LBAR();
      const int n0 = h < 3 ? h * 192 : 448;
      WStream ws; ws.r = mk_rsrc(p.w2, F_ * D_ * 2); pfmt_voff<true>(ws.v, w, lane, 1280); ws.s0 = n0 * 2; ws.step = 64 * 1280;
      proj<true, 24, true>(c, acc, ws, WS_HF + s * 2 * 48 * 1024, 48, ws, false, l16);
#pragma unroll
      for (int T = 0; T < 12; ++T) {
        const u32x2 bv = *(const u32x2*)(p.b2 + n0 + T * 16 + g * 4);
        const f32x4 bb = {b2f(bv[0] & 0xffffu), __uint_as_float(bv[0] & 0xffff0000u), b2f(bv[1] & 0xffffu), __uint_as_float(bv[1] & 0xffff0000u)};
#pragma unroll
        for (int a = 0; a < 2; ++a) {
          f32x4 v = acc[T][a] + bb;
#pragma unroll
          for (int i = 0; i < 4; ++i) v[i] = v[i] / (1.0f + __expf(-v[i]));
          if (h < 3 || T >= 8) st16c(__builtin_bit_cast(u32x4, v), c.ws, l16, WS_G + ((s * 2 + a) * 40 + n0 / 16 + T) * 1024);
        }
      }
      signal_wg(c, C_CG + rb);
      continue;
    }
    WStream ws, nx;
    const unsigned pitch = type == 2 ? 3072 : 12288;
    ws.r = mk_rsrc(type == 0 ? p.Wv : type == 1 ? p.Wk : type == 2 ? p.w1 : p.Wq, D_ * pitch);
    pfmt_voff<false>(ws.v, w, lane, pitch); ws.s0 = h * 384; ws.step = 64 * pitch;
    nx.r = mk_rsrc(p.Wo, D_ * H_ * E_ * 2); ufmt_voff(nx.v, w, lane); nx.s0 = h * 192 * 1280; nx.step = 128;
    proj<false, 10, false>(c, acc, ws, WS_XN + s * 2 * 20 * 1024, 20, nx, type == 0, l16);
    if (type == 0) {
      uphase(c, acc, nx, WS_UX + (s * 40 * 32 + h) * 1024, l16);
      signal_wg(c, C_CU + rb);
    } else if (type == 1) {
#pragma unroll
      for (int a = 0; a < 2; ++a)
#pragma unroll
        for (int m = 0; m < 6; ++m) st16c(__builtin_bit_cast(u32x4, pack8(acc[2 * m][a], acc[2 * m + 1][a])), c.ws, l16, WS_KX + ((s * 32 + h) * 12 + a * 6 + m) * 1024);
      signal_wg(c, C_FK + rb * 32 + h);
    } else if (type == 2) {
#pragma unroll
      for (int T = 0; T < 12; ++T) {
        const u32x2 bv = *(const u32x2*)(p.b1 + h * 192 + T * 16 + g * 4);
        const f32x4 bb = {b2f(bv[0] & 0xffffu), __uint_as_float(bv[0] & 0xffff0000u), b2f(bv[1] & 0xffffu), __uint_as_float(bv[1] & 0xffff0000u)};
#pragma unroll
        for (int a = 0; a < 2; ++a) {
          f32x4 v = acc[T][a] + bb;
#pragma unroll
          for (int i = 0; i < 4; ++i) { const float r = v[i] > 0.f ? v[i] : 0.f; v[i] = r * r; }
          acc[T][a] = v;
        }
      }
#pragma unroll
      for (int a = 0; a < 2; ++a)
#pragma unroll
        for (int m = 0; m < 6; ++m) st16c(__builtin_bit_cast(u32x4, pack8(acc[2 * m][a], acc[2 * m + 1][a])), c.ws, l16, WS_HF + ((s * 2 + a) * 48 + h * 6 + m) * 1024);
      signal_wg(c, C_F1 + rb);
    } else {
      attn_tail(c, acc, s, h, l16);
    }
  }
}

DI void ctx_init(Ctx& c, const Params& p, char* smem) {
  char* base = ws_base(p.ws);
  c.ws = mk_rsrc(base, WS_END);
  c.ctl = (gu32*)(base + WS_CTL);
  c.lds = 0u;
  c.lane = threadIdx.x & 63; c.wave = rfl(threadIdx.x >> 6);
  unsigned pv = c.lane < 24 ? __hip_atomic_load(cw(c, C_PAR + c.lane), RLX_AGENT) : 0u;
  c.par = (unsigned)__ballot(pv != 0);
}
DI int get_role(const Ctx& c, int nwg) {
  LAS unsigned* tk = (LAS unsigned*)(c.lds + LDS_BYTES - 16);
  if (threadIdx.x == 0) {
    int r;
    if (xcc_id() < 4) { r = (int)__hip_atomic_fetch_add(cw(c, C_TKA), 1u, RLX_AGENT); }
    else { r = nwg - 1 - (int)__hip_atomic_fetch_add(cw(c, C_TKB), 1u, RLX_AGENT); }
    *tk = (unsigned)r;
  }
  LBAR();
  int r = (int)rfl(*tk);
  LBAR();
  return r;
}

__global__ void __launch_bounds__(512, 2) k_main(Params p) {
  extern __shared__ __attribute__((aligned(16))) char smem[];
  Ctx c; ctx_init(c, p, smem);
  const int role = get_role(c, 256);
#define ROLE(call) do { asm volatile("v_mbcnt_lo_u32_b32 %0, -1, 0\n\tv_mbcnt_hi_u32_b32 %0, -1, %0" : "=v"(c.lane)); call; STAMP_DUMP(c, role); __builtin_amdgcn_endpgm(); } while (0)
  if (role < 32) { if (c.wave == 0) ROLE(s_wave<0>(p, c, role)); else if (c.wave == 1) ROLE(s_wave<1>(p, c, role)); else if (c.wave < 4) ROLE(s_wave<3>(p, c, role)); else ROLE(s_wave<2>(p, c, role)); }
  else if (role < 64) { if (c.wave < 4) ROLE(v_wave<true>(p, c, role - 32)); else ROLE(v_wave<false>(p, c, role - 32)); }
  else if (role < NCHAIN) ROLE(m_wave(p, c, role - 64));
  else ROLE(bulk(p, c, 0, N_ITEMS));
}

__global__ void __launch_bounds__(256) k_prep(Params p) {
  __shared__ float red[2][4][16];
  char* base = ws_base(p.ws);
  const int b = blockIdx.x, tid = threadIdx.x, lane = tid & 63, w = tid >> 6;
  if (b < 128) {
    const int tok = lane & 15, g = lane >> 4;
    const u32x4* src = (const u32x4*)(p.x + (size_t)(b * 16 + tok) * D_ + w * 160 + g * 8);
    float v[40];
#pragma unroll
    for (int k = 0; k < 5; ++k) { u32x4 d = src[k * 4];
#pragma unroll
      for (int j = 0; j < 4; ++j) { v[k * 8 + 2 * j] = b2f(d[j] & 0xffffu); v[k * 8 + 2 * j + 1] = __uint_as_float(d[j] & 0xffff0000u); } }
    float s = 0.f;
#pragma unroll
    for (int i = 0; i < 40; ++i) s += v[i];
    s = x32sum(x16sum(s));
    if (lane < 16) red[0][w][lane] = s;
    __syncthreads();
    const float mean = (red[0][0][tok] + red[0][1][tok] + red[0][2][tok] + red[0][3][tok]) * (1.0f / D_);
    float q = 0.f;
#pragma unroll
    for (int i = 0; i < 40; ++i) { v[i] -= mean; q += v[i] * v[i]; }
    q = x32sum(x16sum(q));
    if (lane < 16) red[1][w][lane] = q;
    __syncthreads();
    const float var = (red[1][0][tok] + red[1][1][tok] + red[1][2][tok] + red[1][3][tok]) * (1.0f / D_);
    const float rs = 1.0f / sqrtf(var + 1e-5f);
    u32x4* dst = (u32x4*)(base + WS_XN + (size_t)(b * 20 + w * 5) * 1024) + lane;
#pragma unroll
    for (int k = 0; k < 5; ++k) { u32x4 d;
#pragma unroll
      for (int j = 0; j < 4; ++j) d[j] = pk2(v[k * 8 + 2 * j] * rs, v[k * 8 + 2 * j + 1] * rs);
      dst[k * 64] = d; }
  } else if (b < 152) {
    const int pg = b - 128;
    if (w == 0) {
      rsrc_t r = mk_rsrc(base, WS_END);
      const unsigned o = pg * (2 * MB) + (pg < 2 ? 31 * 65536 + 16384 + 3072 : 1920 * KB);
      unsigned best[2] = {~0u, ~0u};
      for (int rep = 0; rep < 6; ++rep)
#pragma unroll
        for (int hh = 0; hh < 2; ++hh) {
          unsigned long long t0 = __builtin_amdgcn_s_memtime();
          st4c(SENT, r, (lane & 31) * 4, o + hh * 8192); WAIT_V(0);
          unsigned long long t1 = __builtin_amdgcn_s_memtime();
          best[hh] = min(best[hh], (unsigned)(t1 - t0));
        }
      if (lane == 0) ((unsigned*)(base + WS_CTL))[(C_PAR + pg) * 32] = (unsigned)(best[1] < best[0]) ^ (unsigned)(xcc_id() >= 4);
    }
  } else if (b == 152) {
    for (int i = tid; i < C_N; i += 256) if (i < C_PAR || i >= C_PAR + 24) ((unsigned*)(base + WS_CTL))[i * 32] = 0u;
    for (int i = tid; i < 8 * 320; i += 256) {
      const int r = i / 320, cp = i % 320, ct = cp >> 3, q = cp & 7;
      unsigned v = ((const unsigned*)p.mem0)[r * 320 + cp]; v = v < 0xFFFFFFFEu ? v : 0xFFFFFFFEu;
      const unsigned o = nmap(ct * 256 + (q & 1) * 128 + r * 16 + (q >> 1) * 4);
      *(unsigned*)(base + WS_XREC + o) = v; *(unsigned*)(base + WS_XREC + o + 8192) = v;
    }
  } else {
    const u32x4 sv = {SENT, SENT, SENT, SENT};
    const int nb = gridDim.x - 153;
    constexpr unsigned n16 = (4 * MB + 1280 * KB) / 16;
    for (unsigned i = (b - 153) * 256 + tid; i < n16; i += nb * 256) {
      const unsigned o = i * 16;
      if (o < 32768 && (o >> 14) * 8192 + (o & 8191u) < MEM_BYTES) continue;
      *(u32x4*)(base + o) = sv;
    }
  }
}

extern "C" void kernel_launch(void* const* d_in, const int* in_sizes, int n_in, void* d_out, int out_size, void* d_ws, size_t ws_size, hipStream_t stream) {
  Params p;
  p.x = (const u16*)d_in[0]; p.mem0 = (const u16*)d_in[1]; p.Wq = (const u16*)d_in[2]; p.Wk = (const u16*)d_in[3]; p.Wv = (const u16*)d_in[4]; p.Wo = (const u16*)d_in[5];
  p.w1 = (const u16*)d_in[8]; p.b1 = (const u16*)d_in[9]; p.w2 = (const u16*)d_in[10]; p.b2 = (const u16*)d_in[11];
  p.out = (u16*)d_out; p.ws = (char*)d_ws;
  hipFuncSetAttribute((const void*)k_main, hipFuncAttributeMaxDynamicSharedMemorySize, LDS_BYTES);
  k_prep<<<256, 256, 0, stream>>>(p);
  k_main<<<256, 512, LDS_BYTES, stream>>>(p);
}
```

```cpp
#include <hip/hip_runtime.h>
#include <hip/hip_bf16.h>
#include <stdint.h>

#define DI __device__ __forceinline__
#define LAS __attribute__((address_space(3)))
#define GAS __attribute__((address_space(1)))
typedef unsigned short u16;
typedef __bf16 bf16x2 __attribute__((ext_vector_type(2)));
typedef __bf16 bf16x4 __attribute__((ext_vector_type(4)));
typedef __bf16 bf16x8 __attribute__((ext_vector_type(8)));
typedef __bf16 bf16x16 __attribute__((ext_vector_type(16)));
typedef float f32x2 __attribute__((ext_vector_type(2)));
typedef float f32x4 __attribute__((ext_vector_type(4)));
typedef unsigned u32x2 __attribute__((ext_vector_type(2)));
typedef unsigned u32x4 __attribute__((ext_vector_type(4)));
typedef __amdgpu_buffer_rsrc_t rsrc_t;
typedef GAS unsigned gu32;
#define RLX_AGENT __ATOMIC_RELAXED, __HIP_MEMORY_SCOPE_AGENT

constexpr int T_ = 2048, D_ = 640, H_ = 32, E_ = 192, F_ = 1536, NS_ = 64;
constexpr unsigned MB = 1u << 20, KB = 1u << 10;
constexpr unsigned WS_XREC = 0;
constexpr unsigned WS_UM = 4 * MB;
constexpr unsigned WS_UX = 48 * MB;
constexpr unsigned WS_KX = 128 * MB;
constexpr unsigned WS_XN = 152 * MB;
constexpr unsigned WS_KM = 156 * MB;
constexpr unsigned WS_PX = 168 * MB;
constexpr unsigned WS_HF = 174 * MB;
constexpr unsigned WS_G = 180 * MB;
constexpr unsigned WS_CTL = 186 * MB;
constexpr unsigned WS_DBG = 187 * MB;
constexpr unsigned WS_END = 190 * MB;
constexpr unsigned MEM_BYTES = 10240, PCM_OFF = 12288, PCX_OFF = 16384;
constexpr unsigned TA_OFF = 10240, TM_OFF = 0, TS_OFF = 512;
#define VIS_S 8
#define VIS_V 8
#define VIS_M 8
#define RST_DLY 24
#define UM_LAST 1
#define XNOW 1
#define XNOX 0
#define XNOS 0
#define FIN_THR 0
#define TOUCH 3
#define XF 0
#define XG 0
#define LEAK 2
#define PRE_S 18
#define PRE_V 12
#define PRE_M 40
constexpr int C_TKA = 0, C_TKB = 1, C_ABORT = 2, C_Q = 3, C_T0 = 4, C_PAR = 8  , C_FK = 32  , C_CU = 288  , C_PS = 296  , C_PV = 328  ,
              C_F1 = 360  , C_CG = 368  , C_CP = 376  , C_N = 440;
constexpr unsigned SENT = 0xFFFFFFFFu;
constexpr unsigned SPIN_CAP = 1u << 18;
constexpr int LDS_BYTES = 160 * 1024;
constexpr int NCHAIN = 104;

struct Params {
  const u16 *x, *mem0, *Wq, *Wk, *Wv, *Wo, *w1, *b1, *w2, *b2;
  u16* out; char* ws;
};

DI rsrc_t mk_rsrc(const void* p, unsigned bytes) { return __builtin_amdgcn_make_buffer_rsrc((void*)p, (short)0, (int)bytes, 0x00020000); }
DI char* ws_base(char* ws) { return (char*)(((uintptr_t)ws + (2 * MB - 1)) & ~(uintptr_t)(2 * MB - 1)); }
DI unsigned rfl(unsigned v) { return __builtin_amdgcn_readfirstlane(v); }
DI unsigned xcc_id() { return (unsigned)__builtin_amdgcn_s_getreg(20 | (3 << 11)) & 0xfu; }
DI unsigned nmap(unsigned o) { return ((o >> 13) << 14) | (o & 8191u); }
DI unsigned pk2(float a, float b) { f32x2 f = {a, b}; return __builtin_bit_cast(unsigned, __builtin_convertvector(f, bf16x2)); }
DI unsigned pk2s(float a, float b) { unsigned v = pk2(a, b); return v < 0xFFFFFFFEu ? v : 0xFFFFFFFEu; }
DI float b2f(unsigned v) { return __uint_as_float(v << 16); }
DI bf16x8 pack8(f32x4 a, f32x4 b) { u32x4 r = {pk2(a[0], a[1]), pk2(a[2], a[3]), pk2(b[0], b[1]), pk2(b[2], b[3])}; return __builtin_bit_cast(bf16x8, r); }
#define WAIT_V(n) asm volatile("s_waitcnt vmcnt(" #n ")" ::: "memory")
#define WAIT_L0() asm volatile("s_waitcnt lgkmcnt(0)" ::: "memory")
#define BAR() do { asm volatile("" ::: "memory"); __builtin_amdgcn_s_barrier(); asm volatile("" ::: "memory"); } while (0)
#define LBAR() do { WAIT_L0(); BAR(); } while (0)
#define MFMA(a, b, c) __builtin_amdgcn_mfma_f32_16x16x32_bf16((a), (b), (c), 0, 0, 0)
#define SMF(a, b, c, i) __builtin_amdgcn_smfmac_f32_16x16x64_bf16((a), (b), (c), (i), 0, 0)

DI void glds(rsrc_t r, unsigned voff, unsigned soff, unsigned ldsdst) {
  asm volatile("s_mov_b32 m0, %0\n\ts_nop 0\n\tbuffer_load_dwordx4 %1, %2, %3 offen lds" ::"s"(ldsdst), "v"(voff), "s"(r), "s"(soff) : "memory");
}
DI bf16x4 trd(unsigned a) { return __builtin_amdgcn_ds_read_tr16_b64_v4bf16((LAS bf16x4*)a); }
DI bf16x8 cat4(bf16x4 a, bf16x4 b) { return __builtin_shufflevector(a, b, 0, 1, 2, 3, 4, 5, 6, 7); }
DI bf16x16 cat8(bf16x8 a, bf16x8 b) { return __builtin_shufflevector(a, b, 0, 1, 2, 3, 4, 5, 6, 7, 8, 9, 10, 11, 12, 13, 14, 15); }
DI bf16x8 ldsr16(unsigned a) { return *(LAS bf16x8*)a; }
DI u32x4 ld16c(rsrc_t r, unsigned v, unsigned s) { return __builtin_amdgcn_raw_buffer_load_b128(r, v, s, 16); }
DI u32x2 ld8c(rsrc_t r, unsigned v, unsigned s) { return __builtin_amdgcn_raw_buffer_load_b64(r, v, s, 16); }
DI unsigned ld4c(rsrc_t r, unsigned v, unsigned s) { return __builtin_amdgcn_raw_buffer_load_b32(r, v, s, 16); }
DI void st16c(u32x4 d, rsrc_t r, unsigned v, unsigned s) { __builtin_amdgcn_raw_buffer_store_b128(d, r, v, s, 16); asm volatile("s_nop 1" ::"v"(d) : "memory"); }
DI void st8c(u32x2 d, rsrc_t r, unsigned v, unsigned s) { __builtin_amdgcn_raw_buffer_store_b64(d, r, v, s, 16); }
DI void st4c(unsigned d, rsrc_t r, unsigned v, unsigned s) { __builtin_amdgcn_raw_buffer_store_b32(d, r, v, s, 16); }
DI unsigned umax3(unsigned a, unsigned b, unsigned c) { return max(max(a, b), c); }
DI unsigned umax4(unsigned m, u32x4 v) { return umax3(umax3(m, v[0], v[1]), v[2], v[3]); }
DI float swp32_sum(float a, float b) {
  u32x2 r = __builtin_amdgcn_permlane32_swap(__float_as_uint(a), __float_as_uint(b), false, false);
  return __uint_as_float(r[0]) + __uint_as_float(r[1]);
}
DI float x16max(float v) { u32x2 r = __builtin_amdgcn_permlane16_swap(__float_as_uint(v), __float_as_uint(v), false, false); return fmaxf(__uint_as_float(r[0]), __uint_as_float(r[1])); }
DI float x32max(float v) { u32x2 r = __builtin_amdgcn_permlane32_swap(__float_as_uint(v), __float_as_uint(v), false, false); return fmaxf(__uint_as_float(r[0]), __uint_as_float(r[1])); }
DI float x16sum(float v) { u32x2 r = __builtin_amdgcn_permlane16_swap(__float_as_uint(v), __float_as_uint(v), false, false); return __uint_as_float(r[0]) + __uint_as_float(r[1]); }
DI float x32sum(float v) { u32x2 r = __builtin_amdgcn_permlane32_swap(__float_as_uint(v), __float_as_uint(v), false, false); return __uint_as_float(r[0]) + __uint_as_float(r[1]); }
DI unsigned dpp_shr8(unsigned keep, unsigned v) { return __builtin_amdgcn_update_dpp(keep, v, 0x118, 0xf, 0xf, false); }
DI unsigned dpp_x1(unsigned v) { return __builtin_amdgcn_update_dpp(v, v, 0xB1, 0xf, 0xf, false); }

struct Ctx {
  rsrc_t ws; gu32* ctl; unsigned lds; int lane, wave; unsigned par, t0;
};
DI int now(const Ctx& c) { return (int)(((unsigned)__builtin_amdgcn_s_memrealtime() - c.t0) & 0x7FFFFFFFu); }
DI void wait_until(const Ctx& c, int t) { for (int i = 0; i < 4096 && now(c) < t; ++i) {} }
DI int wave_max(int v) {
  unsigned u = (unsigned)v;
  u = max(u, (unsigned)__builtin_amdgcn_update_dpp(0, u, 0x111, 0xf, 0xf, false)); u = max(u, (unsigned)__builtin_amdgcn_update_dpp(0, u, 0x112, 0xf, 0xf, false));
  u = max(u, (unsigned)__builtin_amdgcn_update_dpp(0, u, 0x114, 0xf, 0xf, false)); u = max(u, (unsigned)__builtin_amdgcn_update_dpp(0, u, 0x118, 0xf, 0xf, false));
  return (int)max(max((unsigned)__builtin_amdgcn_readlane(u, 15), (unsigned)__builtin_amdgcn_readlane(u, 31)), max((unsigned)__builtin_amdgcn_readlane(u, 47), (unsigned)__builtin_amdgcn_readlane(u, 63)));
}
DI gu32* cw(const Ctx& c, int i) { return c.ctl + i * 32; }
DI bool aborted(const Ctx& c) { return rfl(__hip_atomic_load(cw(c, C_ABORT), RLX_AGENT)) != 0; }
DI void do_abort(const Ctx& c, unsigned code) { if (c.lane == 0) __hip_atomic_store(cw(c, C_ABORT), code, RLX_AGENT); }
DI bool spin_fail(const Ctx& c, unsigned& spins, unsigned code) {
  ++spins;
  if (spins > SPIN_CAP) { do_abort(c, code); return true; }
  return (spins & 255u) == 0u && aborted(c);
}
DI void wait_ge(const Ctx& c, int i, unsigned want, unsigned code, bool lazy = false) {
  for (unsigned spins = 0;;) {
    const unsigned v = rfl(__hip_atomic_load(cw(c, i), RLX_AGENT));
    if (v >= want) break;
    if (lazy && v + 1 < want) __builtin_amdgcn_s_sleep(32); else __builtin_amdgcn_s_sleep(2);
    if (spin_fail(c, spins, code)) break;
  }
  asm volatile("" ::: "memory");
}
DI unsigned xrec_soff(const Ctx& c, int s) { return WS_XREC + (unsigned)s * 65536u + (((c.par >> (s >> 5)) & 1u) << 13); }
DI unsigned far_soff(const Ctx& c, int s) { return (xrec_soff(c, s) ^ 8192u) + 32768u; }
DI unsigned um_soff(const Ctx& c, int s) { int pg = s / 3; return WS_UM + (unsigned)pg * (2 * MB) + (unsigned)(s - pg * 3) * (640 * KB) + (((c.par >> (2 + pg)) & 1u) << 13); }

template <bool PI> DI void pfmt_voff(unsigned (&v)[3], int wave, int lane, unsigned pitch) {
#pragma unroll
  for (int i = 0; i < 3; ++i) {
    unsigned n = (wave * 3 + i) * 64 + lane, row = n / 24, pos = n % 24;
    unsigned key = PI ? ((row >> 1) & 3) : (((row >> 1) & 1) | (((row >> 3) & 1) << 1));
    v[i] = row * pitch + ((((pos >> 1) ^ key) << 1) | (pos & 1)) * 16;
  }
}
DI void ufmt_voff(unsigned (&v)[3], int wave, int lane) {
#pragma unroll
  for (int i = 0; i < 3; ++i) {
    unsigned n = (wave * 3 + i) * 64 + lane, row = n >> 3, pos = n & 7;
    v[i] = row * 1280u + ((((pos >> 1) ^ ((row >> 1) & 3)) << 1) | (pos & 1)) * 16;
  }
}
DI void stage_load(rsrc_t r, const unsigned (&v)[3], unsigned soff, unsigned slot, int wave) {
#pragma unroll
  for (int i = 0; i < 3; ++i) glds(r, v[i], soff, slot + (unsigned)(wave * 3 + i) * 1024u);
}
DI unsigned lane_kx(int lane) { return (((lane >> 4) & 1) << 1) | ((lane >> 3) & 1); }
template <bool PI> DI unsigned pfmt_base(int lane, int T) { int g = lane >> 4, q = (lane >> 2) & 3; return (unsigned)(((PI ? 4 : 8) * g + q) * 384 + ((T ^ lane_kx(lane)) * 32) + (lane & 3) * 8); }
DI unsigned ufmt_base(int lane, int C) { int g = lane >> 4, q = (lane >> 2) & 3; return (unsigned)((4 * g + q) * 128 + ((C ^ lane_kx(lane)) * 32) + (lane & 3) * 8); }
template <bool PI> DI bf16x8 pfmt_frag(unsigned a, int ks) { return cat4(trd(a + ks * 32 * 384), trd(a + ks * 32 * 384 + (PI ? 16 : 4) * 384)); }
DI bf16x8 ufmt_frag(unsigned a, int ks) { return cat4(trd(a + ks * 32 * 128), trd(a + ks * 32 * 128 + 16 * 128)); }
DI bf16x16 pfmt_frag64(unsigned a) { return cat8(pfmt_frag<false>(a, 0), pfmt_frag<false>(a, 1)); }
DI bf16x16 ufmt_frag64(unsigned a, int kk) { return cat8(ufmt_frag(a, 2 * kk), ufmt_frag(a, 2 * kk + 1)); }

constexpr unsigned L_MEM = 120 * KB, L_Q = 130 * KB, L_K = 136 * KB, L_KX = 142 * KB, L_MISC = 154 * KB;
DI constexpr unsigned slot_off(int i) { return i == 0 ? 0u : i == 1 ? 72 * KB : i == 2 ? 24 * KB : i == 3 ? 120 * KB : i == 4 ? 48 * KB : 96 * KB; }
#define OPQ(x) asm volatile("" : "+v"(x))
#define SB() __builtin_amdgcn_sched_barrier(0)
#define XPIN2(x) asm volatile("" : "+v"(x))

#define STAMP(c, role, s, k) do {} while (0)
#define STAMP_DECL do {} while (0)
#define FAILS(w) do {} while (0)
#define FAIL0(s) do {} while (0)
#define STAMP_FLUSH(cond, s, mask) do {} while (0)
#define STAMP_DUMP(c, role) do {} while (0)
#define TS_ROLE 0
#define TSD unsigned long long tt[10] = {}
#define TS(r, i) do { if (TS_ROLE == (r)) { SB(); asm volatile("s_memtime %0" : "=s"(tt[i])); SB(); } } while (0)
#define TSE(r) do { if (TS_ROLE == (r)) { asm volatile("s_waitcnt lgkmcnt(0)" : "+s"(tt[0]), "+s"(tt[1]), "+s"(tt[2]), "+s"(tt[3]), "+s"(tt[4]), "+s"(tt[5]), "+s"(tt[6]), "+s"(tt[7]), "+s"(tt[8]), "+s"(tt[9])); \
    if (s == 50 && h == 5) { for (int j = 0; j < 10; ++j) ((GAS unsigned*)((char*)c.ctl + (WS_DBG - WS_CTL) + MB))[w * 16 + j] = tt[j] ? (unsigned)(tt[j] - tt[0]) : 0u; } } } while (0)
constexpr unsigned L_STAMP = 157 * KB + 512;

#define POLL(c, bud, code, ...) do { bool ok; \
    _Pragma("nounroll") do { asm volatile("" ::: "memory"); __VA_ARGS__; } while (!ok && --(bud) > 0); \
    if (__builtin_expect(!ok, 0)) do_abort(c, code); } while (0)
DI void poll_mem(const Ctx& c, int& bud, int s, unsigned l16, int u, unsigned la) {
  const unsigned so = xrec_soff(c, s) + u * 1024;
  la += u * 1024;
  if (u < 2) {
    POLL(c, bud, 0x100 + s, { u32x4 a = ld16c(c.ws, l16, so); u32x4 b = ld16c(c.ws, l16, so + 4096); u32x4 d = ld16c(c.ws, l16, so + 16384);
      *(LAS u32x4*)(la) = a; *(LAS u32x4*)(la + 4096) = b; *(LAS u32x4*)(la + 8192) = d;
      ok = __all(umax4(umax4(umax4(0u, a), b), d) != SENT); });
  } else {
    POLL(c, bud, 0x100 + s, { u32x4 a = ld16c(c.ws, l16, so); u32x4 b = ld16c(c.ws, l16, so + 4096);
      *(LAS u32x4*)(la) = a; *(LAS u32x4*)(la + 4096) = b;
      ok = __all(umax4(umax4(0u, a), b) != SENT); });
  }
}
DI int last_pub(const Ctx& c, int s, unsigned off, unsigned n, unsigned l16) { OPQ(l16); return wave_max((int)ld4c(c.ws, min(l16 >> 2, (n - 1) * 4), far_soff(c, s) + off)); }
DI int last_arr(const Ctx& c, int s, unsigned l16) { OPQ(l16); return wave_max((int)ld4c(c.ws, (l16 << 2) & 2047u, xrec_soff(c, s) + nmap(TA_OFF))); }
DI int wait_arr(const Ctx& c, int s, unsigned l16) {
  int t = last_arr(c, s, l16);
  for (int i = 0; i < 64 && t < 0; ++i) { asm volatile("" ::: "memory"); __builtin_amdgcn_s_sleep(2); t = last_arr(c, s, l16); }
  return t;
}
DI int pace_sv(const Ctx& c, int s, int& ta0, int& ze, unsigned l16, int vis) {
  if (s + 1 >= NS_) return 0;
  OPQ(l16);
  const unsigned vm = ld4c(c.ws, min(l16 >> 2, 156u), far_soff(c, s) + TM_OFF), va = ld4c(c.ws, (l16 << 2) & 2047u, xrec_soff(c, s) + nmap(TA_OFF));
  const int tm = wave_max((int)vm);
  if (tm >= 0 && ta0 >= 0) ze = min(tm - ta0, ze + LEAK);
  ta0 = wave_max((int)va);
  if (ta0 < 0) ta0 = wait_arr(c, s, l16);
  return ta0 >= 0 && ze < 400 ? ta0 + ze + vis : 0;
}
DI bf16x16 ldsr32(unsigned a) { return cat8(ldsr16(a), ldsr16(a + 1024)); }
DI void ldsw32(unsigned a, bf16x16 v) {
  *(LAS bf16x8*)a = __builtin_shufflevector(v, v, 0, 1, 2, 3, 4, 5, 6, 7); *(LAS bf16x8*)(a + 1024) = __builtin_shufflevector(v, v, 8, 9, 10, 11, 12, 13, 14, 15);
}
DI f32x2 hsum(f32x4 v) { float a = v[0] + v[2], b = v[1] + v[3]; OPQ(a); OPQ(b); return f32x2{a, b}; }
DI f32x2 smul(float a, float b, float k) { a *= k; b *= k; OPQ(a); OPQ(b); return f32x2{a, b}; }
DI float x2(float v) { return __uint_as_float(__builtin_amdgcn_update_dpp(0u, __float_as_uint(v), 0x4E, 0xf, 0xf, true)); }

template <int KIND> DI void s_wave(const Params& p, const Ctx& c, int h) {
  constexpr bool QW = KIND != 2;
  constexpr int NR = QW ? 7 : 8;
  constexpr unsigned LB = QW ? 0 : 72 * KB;
  const int lane = c.lane, w = c.wave, u = w & 3;
  bf16x16 W[NR][3];
  unsigned wl = LB + u * 6144 + lane * 16; OPQ(wl);
  {
    const unsigned tl = pfmt_base<false>(lane, u);
    rsrc_t rq = mk_rsrc(p.Wq, D_ * H_ * E_ * 2), rk = mk_rsrc(p.Wk, D_ * H_ * E_ * 2);
    unsigned v[3]; pfmt_voff<false>(v, w, lane, 12288);
    const unsigned s0 = h * 384;
#pragma unroll
    for (int i = 0; i < 2; ++i) { stage_load(rq, v, s0 + i * 64 * 12288, slot_off(2 * i), w); stage_load(rk, v, s0 + i * 64 * 12288, slot_off(2 * i + 1), w); }
#pragma unroll
    for (int i = 0; i < 10; ++i) {
      if (i < 9) WAIT_V(6); else WAIT_V(0);
      LBAR();
      if (i + 2 < 10) { stage_load(rq, v, s0 + (i + 2) * 64 * 12288, slot_off(2 * ((i + 2) % 3)), w); stage_load(rk, v, s0 + (i + 2) * 64 * 12288, slot_off(2 * ((i + 2) % 3) + 1), w); }
      const unsigned so = slot_off(2 * (i % 3) + (QW ? 0 : 1));
      bf16x16 f[3];
#pragma unroll
      for (int t = 0; t < 3; ++t) f[t] = pfmt_frag64(tl + t * 128 + so);
      if (i >= 7) LBAR();
#pragma unroll
      for (int t = 0; t < 3; ++t) { if (i < NR) W[i < NR ? i : 0][t] = f[t]; else ldsw32(wl + so - LB + t * 2048, f[t]); }
    }
    LBAR();
  }
  const int idx = (lane & 2) ? 0xEEEE : 0x4444;
  int bud = 1 << 20; STAMP_DECL;
  const int g = lane >> 4, r16 = lane & 15;
  unsigned l16 = lane * 16; OPQ(l16); l16 &= 0x3F0u;
  unsigned memr = L_MEM + lane * 16; OPQ(memr);
  unsigned qkw = (QW ? L_Q : L_K) + (16 * u + r16) * 32 + ((g ^ (r16 >> 2)) * 8); OPQ(qkw);
  unsigned qkr = L_Q + (4 * g + (r16 >> 2)) * 32 + (((lane & 3) ^ g) * 8);
  unsigned kxa = L_KX + ((r16 >> 2) & 1) * 6144 + (g * 16 + 8 * u + 4 * (r16 >> 3) + (r16 & 3)) * 16;
  unsigned pxo = nmap(PCX_OFF + h * 512) + u * 256 + r16 * 16 + g * 4, pmo = nmap(PCM_OFF + h * 128) + ((r16 & 13) | ((g & 1) << 1)) * 8 + (g >> 1) * 4;
  unsigned exw = L_MISC + u * 1024 + lane * 16;
  if (KIND < 2) OPQ(qkr);
  if (KIND == 0) { OPQ(kxa); OPQ(pxo); OPQ(pmo); OPQ(exw); }
  volatile LAS unsigned* qflag = (volatile LAS unsigned*)(L_MISC + 2048);
  if (QW) qflag[u] = 0;
  const unsigned kxw = memr + (L_KX - L_MEM);
  if (KIND == 3) {
    wait_ge(c, C_FK + h, 1, 0x200);
#pragma unroll
    for (int i = 0; i < 12; ++i) *(LAS u32x4*)(kxw + i * 1024) = ld16c(c.ws, l16, WS_KX + (h * 12 + i) * 1024);
  }
  const bool hi2 = lane & 2;
  int shot = 0, ref0 = -1, y0p = 1 << 20;
  const float sc = 0.07216878364870322f * 1.4426950408889634f;
  for (int s = 0; s < NS_; ++s) {
    bf16x16 bt[3];
#pragma unroll
    for (int t = 0; t < 3; ++t) bt[t] = ldsr32(wl + slot_off(2 * (NR % 3) + (QW ? 0 : 1)) - LB + t * 2048);
    f32x4 acc[3] = {};
    OPQ(acc[0]); OPQ(acc[1]); OPQ(acc[2]);
    unsigned so = xrec_soff(c, s); asm volatile("" : "+s"(so));
    SB();
    if (!QW) { wait_until(c, shot); STAMP(c, h, s, 3); poll_mem(c, bud, s, l16, u, memr); STAMP(c, h, s, 4); }
    if (KIND == 1) WAIT_V(0);
    TSD;
    LBAR();
    TS(1, 0);
    if (KIND == 2 && w == 7) st4c((unsigned)now(c), c.ws, 0, so + nmap(TA_OFF) + h * 64);
    {
      constexpr int ord[10] = {NR, 0, 1, 2, QW ? 8 : 3, QW ? 3 : 4, QW ? 4 : 5, QW ? 5 : 6, QW ? 6 : 7, 9};
      constexpr int AWIN = QW ? 4 : 3;
      bf16x8 a[10];
#pragma unroll
      for (int i = 0; i < AWIN; ++i) { a[i] = ldsr16(memr + ord[i] * 1024); SB(); }
      STAMP(c, h, s, 0);
#pragma unroll
      for (int i = 0; i < 10; ++i) {
        const int ks = ord[i];
#pragma unroll
        for (int t = 0; t < 3; ++t) {
          acc[t] = SMF(a[i], ks < NR ? W[ks < NR ? ks : 0][t] : bt[t], acc[t], idx);
          OPQ(acc[t]);
          SB();
          if (ks >= NR && ks < 9) { bt[t] = ldsr32(wl + slot_off(2 * ((ks + 1) % 3) + (QW ? 0 : 1)) - LB + t * 2048); SB(); }
        }
        if (i + AWIN < 10) { a[i + AWIN] = ldsr16(memr + ord[i + AWIN] * 1024); SB(); }
      }
    }
    TS(1, 1);
#pragma unroll
    for (int t = 0; t < 3; ++t) { const f32x2 r = hsum(acc[t]); const unsigned d = pk2(r[0], r[1]); *(LAS u32x2*)(qkw + t * 2048) = u32x2{d, d}; }
    if (QW) { asm volatile("" ::: "memory"); qflag[u] = (unsigned)(s + 1); }
    TS(1, 2);
    bf16x8 qf[6]; f32x2 y, ex; float lx;
    u32x4 kxn[12];
    if (KIND == 0) {
      bf16x8 kx[6];
      SB();
#pragma unroll
      for (int m = 0; m < 6; ++m) kx[m] = ldsr16(kxa + m * 1024);
      SB();
      for (int sp = 0; sp < (1 << 22); ++sp) { const u32x4 f = *(volatile LAS u32x4*)qflag; if (rfl(min(min(f[0], f[1]), min(f[2], f[3]))) > (unsigned)s) break; }
      asm volatile("" ::: "memory");
      TS(1, 3);
#pragma unroll
      for (int m = 0; m < 6; ++m) qf[m] = cat4(trd(qkr + m * 1024), trd(qkr + m * 1024 + 512));
      SB();
      f32x4 sx = {};
#pragma unroll
      for (int m = 0; m < 6; ++m) sx = MFMA(kx[m], qf[m], sx);
      OPQ(sx); TS(1, 4);
      y = smul(hi2 ? sx[2] : sx[0], hi2 ? sx[3] : sx[1], sc);
      ex = f32x2{__builtin_amdgcn_exp2f(y[0]), __builtin_amdgcn_exp2f(y[1])};
      lx = ex[0] + ex[1];
      f32x4 o = {lx, y[0], y[1], lx};
      *(LAS f32x4*)exw = o;
    }
    if (KIND == 1) { if (lane == 0 && s > 0) __hip_atomic_store(cw(c, C_PS + h), (unsigned)s, RLX_AGENT); }
    if (KIND == 3) {
      const int sn = s + 1 < NS_ ? s + 1 : s;
#pragma unroll
      for (int i = 0; i < 12; ++i) kxn[i] = ld16c(c.ws, l16, WS_KX + ((sn * 32 + h) * 12 + i) * 1024);
    }
    TS(1, 5);
    LBAR();
    TS(1, 6);
    if (!QW) shot = pace_sv(c, s, ref0, y0p, l16, VIS_S);
    if (KIND == 0) {
      if (s + 1 < NS_) {
        STAMP(c, h, s, 1);
        bf16x8 kf[6];
#pragma unroll
        for (int m = 0; m < 6; ++m) { kf[m] = cat4(trd(qkr + (L_K - L_Q) + m * 1024), trd(qkr + (L_K - L_Q) + m * 1024 + 512)); SB(); }
        const float lo = *(LAS float*)(exw ^ 1024);
        SB();
        f32x4 sm = {};
#pragma unroll
        for (int m = 0; m < 6; ++m) sm = MFMA(kf[m], qf[m], sm);
        lx += lo; OPQ(lx); OPQ(sm); TS(1, 7);
        const f32x2 z = smul(sm[0], sm[1], sc);
        f32x2 em = {__builtin_amdgcn_exp2f(z[0]), __builtin_amdgcn_exp2f(z[1])};
        float l = lx + 0.5f * (em[0] + em[1]); l += x2(l); l = x32sum(x16sum(l));
        if (__builtin_expect(!__all(__float_as_uint(l) - 0x21800000u < 0x50000000u), 0)) {
          const f32x4 o = *(LAS f32x4*)(exw ^ 1024);
          float mx = fmaxf(fmaxf(fmaxf(y[0], y[1]), fmaxf(o[1], o[2])), fmaxf(z[0], z[1]));
          mx = fmaxf(mx, x2(mx)); mx = x32max(x16max(mx));
          ex[0] = __builtin_amdgcn_exp2f(y[0] - mx); ex[1] = __builtin_amdgcn_exp2f(y[1] - mx);
          em[0] = __builtin_amdgcn_exp2f(z[0] - mx); em[1] = __builtin_amdgcn_exp2f(z[1] - mx);
          l = ((ex[0] + ex[1]) + (__builtin_amdgcn_exp2f(o[1] - mx) + __builtin_amdgcn_exp2f(o[2] - mx))) + 0.5f * (em[0] + em[1]);
          l += x2(l); l = x32sum(x16sum(l));
          if (!(l == l)) { l = 1.0f; ex[0] = ex[1] = em[0] = em[1] = __uint_as_float(0x7FC00000u); }
        }
        const float inv = __builtin_amdgcn_rcpf(l);
        ex = smul(ex[0], ex[1], inv);
        st4c(pk2(ex[0], ex[1]), c.ws, pxo, so);
        if (u == 1) {
          em = smul(em[0], em[1], inv);
          st4c(pk2(em[0], em[1]), c.ws, pmo, so);
          st4c((unsigned)now(c), c.ws, 0, far_soff(c, s) + TS_OFF + h * 4);
        }
        STAMP(c, h, s, 2);
        TS(1, 8);
      }
    }
    if (KIND == 1) {
      __builtin_amdgcn_s_sleep(4);
#pragma unroll
      for (int m = 0; m < 6; ++m) st16c(__builtin_bit_cast(u32x4, cat4(trd(qkr + (L_K - L_Q) + m * 1024), trd(qkr + (L_K - L_Q) + m * 1024 + 512))), c.ws, l16, WS_KM + ((s * 32 + h) * 6 + m) * 1024);
      if (TOUCH && s + TOUCH < NS_ - 1) {
        unsigned tv = l16; OPQ(tv);
        tv = tv < 512 ? nmap(PCX_OFF + h * 512) + tv : tv < 640 ? nmap(PCM_OFF + h * 128) + tv - 512 : nmap(TA_OFF + h * 64) + ((tv - 640) & 63u);
        st16c(u32x4{SENT, SENT, SENT, SENT}, c.ws, tv, xrec_soff(c, s + TOUCH));
      }
    }
    if (KIND == 3) {
      __builtin_amdgcn_s_sleep(4);
#pragma unroll
      for (int i = 0; i < 12; ++i) *(LAS u32x4*)(kxw + i * 1024) = kxn[i];
      if (((s + 2) & 7) == 0 && s + 2 < NS_) wait_ge(c, C_FK + ((s + 2) >> 3) * 32 + h, 1, 0x201);
    }
    TSE(1);
    FAIL0(s); STAMP_FLUSH(w == 0 || w == 4, s, (QW ? 7 : 24));
  }
  FAILS(w);
  if (KIND == 1) { WAIT_V(0); if (lane == 0) __hip_atomic_store(cw(c, C_PS + h), (unsigned)NS_, RLX_AGENT); }
}

template <bool AW> DI void v_wave(const Params& p, const Ctx& c, int h) {
  constexpr int N1 = AW ? 2 : 1, N2R = AW ? 2 : 5;
  const int lane = c.lane, w = c.wave, u = w & 3;
  bf16x16 W1[8][N1], W2[N2R][3];
  unsigned w1l = 72 * KB + (AW ? u * 4096 : 16384 + u * 2048) + lane * 16; OPQ(w1l);
  unsigned w2l = u * 6144 + lane * 16;
  if (AW) OPQ(w2l);
  {
    const unsigned tl = pfmt_base<false>(lane, AW ? u : u + 8);
    const int gb = lane >> 4, qq = (lane >> 2) & 3;
    const unsigned ur = (32 * (gb & 1) + 8 * (gb >> 1) + 2 * (qq & 1) + (qq >> 1)) * 128 + (lane & 3) * 8;
    const unsigned ul0 = ur + ((u ^ (qq & 1)) * 32), ul1 = ur + ((u ^ (2 + (qq & 1))) * 32) + 4 * 128;
    rsrc_t rv = mk_rsrc(p.Wv, D_ * H_ * E_ * 2), ro = mk_rsrc(p.Wo, D_ * H_ * E_ * 2);
    unsigned v[3], vu[3]; pfmt_voff<false>(v, w, lane, 12288); ufmt_voff(vu, w, lane);
    const unsigned s0 = h * 384, u0 = h * 192 * 1280;
#pragma unroll
    for (int i = 0; i < 2; ++i) { stage_load(ro, vu, u0 + i * 128, slot_off(2 * i), w); stage_load(rv, v, s0 + i * 64 * 12288, slot_off(2 * i + 1), w); }
#pragma unroll
    for (int i = 0; i < 10; ++i) {
      if (i < 9) WAIT_V(6); else WAIT_V(0);
      LBAR();
      if (i + 2 < 10) { stage_load(ro, vu, u0 + (i + 2) * 128, slot_off(2 * ((i + 2) % 3)), w); stage_load(rv, v, s0 + (i + 2) * 64 * 12288, slot_off(2 * ((i + 2) % 3) + 1), w); }
      bf16x16 f1[N1], f2[3];
      const bool mine = AW ? i >= 5 : i < 5;
#pragma unroll
      for (int t = 0; t < N1; ++t) f1[t] = pfmt_frag64(tl + t * 128 + slot_off(2 * (i % 3) + 1));
      if (mine) {
#pragma unroll
        for (int kk = 0; kk < 3; ++kk) { const unsigned o = slot_off(2 * (i % 3)) + kk * 64 * 128; f2[kk] = cat8(cat4(trd(ul0 + o), trd(ul1 + o)), cat4(trd(ul0 + o + 16 * 128), trd(ul1 + o + 16 * 128))); }
      }
      if (i >= 7) LBAR();
#pragma unroll
      for (int t = 0; t < N1; ++t) { if (i < 8) W1[i < 8 ? i : 0][t] = f1[t]; else ldsw32(w1l + slot_off(2 * (i % 3) + 1) - 72 * KB + t * 2048, f1[t]); }
      if (mine) {
#pragma unroll
        for (int kk = 0; kk < 3; ++kk) { if (i < 7) W2[AW ? (i == 5 ? 0 : 1) : (i < 5 ? i : 0)][kk] = f2[kk]; else ldsw32(w2l + slot_off(2 * (i % 3)) + kk * 2048, f2[kk]); }
      }
    }
    LBAR();
  }
  const int idx = (lane & 2) ? 0xEEEE : 0x4444;
  int bud = 1 << 20; STAMP_DECL;
  const int g = lane >> 4, r16 = lane & 15;
  unsigned l16 = lane * 16; OPQ(l16); l16 &= 0x3F0u;
  unsigned memr = L_MEM + lane * 16; OPQ(memr);
  unsigned vw = L_Q + ((AW ? u : u + 8) * 8 + (r16 >> 1)) * 32 + g * 8 + (r16 & 1) * 4; OPQ(vw);
  unsigned vr = L_Q + (4 * g + (r16 >> 2)) * 32 + (lane & 3) * 8; OPQ(vr);
  unsigned stv = h * 256 + r16 * 16 + g * 65536 + u * 16384 + (AW ? 5 * 65536 : 0); OPQ(stv);
  unsigned st1 = h * 256 + r16 * 16 + g * 4 + u * 16384 + (AW ? 9 * 65536 : 4 * 65536); OPQ(st1);
  int shot = 0, tm0 = -1, y0p = 1 << 20;
  for (int s = 0; s < NS_; ++s) {
    bf16x16 bt[N1];
#pragma unroll
    for (int t = 0; t < N1; ++t) bt[t] = ldsr32(w1l + slot_off(5) - 72 * KB + t * 2048);
    f32x4 acc[N1] = {};
#pragma unroll
    for (int t = 0; t < N1; ++t) OPQ(acc[t]);
    unsigned so = um_soff(c, s); asm volatile("" : "+s"(so));
    f32x4 oz[5] = {};
#pragma unroll
    for (int j = 0; j < 5; ++j) OPQ(oz[j]);
    SB();
    if (!AW) { wait_until(c, shot); poll_mem(c, bud, s, l16, u, memr); }
    WAIT_V(0);
    TSD;
    LBAR();
    TS(2, 0);
    {
      constexpr int ord[10] = {8, 0, 1, 2, 3, 4, 5, 6, 7, 9};
      constexpr int AWIN = AW ? 4 : 6;
      bf16x8 a[10];
#pragma unroll
      for (int i = 0; i < AWIN; ++i) { a[i] = ldsr16(memr + ord[i] * 1024); SB(); }
      STAMP(c, 32 + h, s, 0);
#pragma unroll
      for (int i = 0; i < 10; ++i) {
        const int ks = ord[i];
#pragma unroll
        for (int t = 0; t < N1; ++t) {
          acc[t] = SMF(a[i], ks < 8 ? W1[ks < 8 ? ks : 0][t] : bt[t], acc[t], idx);
          OPQ(acc[t]); SB();
          if (ks == 8) { bt[t] = ldsr32(w1l + slot_off(1) - 72 * KB + t * 2048); SB(); }
        }
        if (i + AWIN < 10) { a[i + AWIN] = ldsr16(memr + ord[i + AWIN] * 1024); SB(); }
      }
    }
    TS(2, 1);
#pragma unroll
    for (int t = 0; t < N1; ++t) { const f32x2 r = hsum(acc[t]); *(LAS unsigned*)(vw + t * 1024) = pk2(r[0], r[1]); }
    SB();
    TS(2, 2);
    bf16x16 ut[3];
    if (AW) {
#pragma unroll
      for (int kk = 0; kk < 3; ++kk) ut[kk] = ldsr32(w2l + slot_off(2) + kk * 2048);
    }
    if (AW && w == 0 && lane == 0 && s > 0) __hip_atomic_store(cw(c, C_PV + h), (unsigned)s, RLX_AGENT);
    TS(2, 3);
    LBAR();
    TS(2, 4);
    bf16x8 va[3];
#pragma unroll
    for (int kk = 0; kk < 3; ++kk) { va[kk] = cat4(trd(vr + kk * 1024), trd(vr + kk * 1024 + 512)); SB(); }
    SB();
    auto tile = [&](const bf16x16 (&b)[3], int j) { f32x4 o = oz[j];
#pragma unroll
      for (int kk = 0; kk < 3; ++kk) o = SMF(va[kk], b[kk], o, idx);
      OPQ(o); SB();
      return o; };
    auto refill = [&](unsigned off) {
#pragma unroll
      for (int kk = 0; kk < 3; ++kk) ut[kk] = ldsr32(w2l + off + kk * 2048);
      SB(); };
    auto pk = [&](f32x4 o) { const f32x2 r = hsum(o); unsigned d = pk2s(r[0], r[1]); OPQ(d); SB(); return d; };
    auto put4 = [&](unsigned d0, unsigned d1, unsigned d2, unsigned d3) {
      const u32x2 x = __builtin_amdgcn_permlane16_swap(d0, d1, false, false), y = __builtin_amdgcn_permlane16_swap(d2, d3, false, false);
      const u32x2 e = __builtin_amdgcn_permlane32_swap(x[0], y[0], false, false), f = __builtin_amdgcn_permlane32_swap(x[1], y[1], false, false);
      st16c(u32x4{e[0], f[0], e[1], f[1]}, c.ws, stv, so); SB(); };
    if (AW) {
      f32x4 o7 = tile(ut, 0); refill(slot_off(4));
      f32x4 o5 = tile(W2[0], 1); const unsigned d7 = pk(o7);
      f32x4 o6 = tile(W2[1], 2); const unsigned d5 = pk(o5);
      f32x4 o8 = tile(ut, 3); refill(slot_off(0)); const unsigned d6 = pk(o6), d8 = pk(o8);
      f32x4 o9 = tile(ut, 4);
      TS(2, 5);
      put4(d5, d6, d7, d8);
      st4c(pk(o9), c.ws, st1, so);
    } else {
      f32x4 o4 = tile(W2[AW ? 0 : 4], 4);
      f32x4 o0 = tile(W2[0], 0); st4c(pk(o4), c.ws, st1, so);
      f32x4 o1 = tile(W2[1], 1); const unsigned d0 = pk(o0);
      f32x4 o2 = tile(W2[AW ? 0 : 2], 2); const unsigned d1 = pk(o1);
      f32x4 o3 = tile(W2[AW ? 0 : 3], 3); const unsigned d2 = pk(o2);
      TS(2, 5);
      put4(d0, d1, d2, pk(o3));
    }
    STAMP(c, 32 + h, s, 2);
    TS(2, 7); TSE(2);
    if (!AW) shot = pace_sv(c, s, tm0, y0p, l16, VIS_V);
    if (AW && s + 2 < NS_) {
      __builtin_amdgcn_s_sleep(RST_DLY);
      const unsigned sr = um_soff(c, s + 2) + h * 256 + u * 16384; const u32x4 d = {SENT, SENT, SENT, SENT};
      unsigned rv = l16; OPQ(rv); rv = (rv & 255u) + (rv >> 8) * 65536;
      st16c(d, c.ws, rv, sr); st16c(d, c.ws, rv, sr + 4 * 65536); if (lane < 32) st16c(d, c.ws, rv, sr + 8 * 65536);
    }
    FAIL0(s); STAMP_FLUSH(w == 4, s, 5);
  }
  FAILS(w);
  WAIT_V(0);
  BAR();
  if (w == 0 && lane == 0) __hip_atomic_store(cw(c, C_PV + h), (unsigned)NS_, RLX_AGENT);
}

DI void m_wave(const Params& p, const Ctx& c, int ct) {
  const int lane = c.lane, w = c.wave;
  const int idx = (lane & 2) ? 0xEEEE : 0x4444;
  int bud = 1 << 20; STAMP_DECL;
  const int g = lane >> 4, r16 = lane & 15;
  unsigned l16 = lane * 16; OPQ(l16); l16 &= 0x3F0u;
  unsigned vm = g * 256 + r16 * 8; OPQ(vm); vm &= 0x378u;
  unsigned vpub = nmap(ct * 256) + (4 * g + (r16 & 3)) * 16 + (r16 >> 2) * 4; OPQ(vpub);
  volatile LAS unsigned* done = (volatile LAS unsigned*)(64);
  if (w == 0) { done[0] = 0; done[1] = ~0u; done[2] = 1 << 20; done[3] = 1 << 20; }
  LBAR();
  for (int s = w; s < NS_ - 1; s += 8) {
    u32x4 bx[32], bm[8];
    wait_ge(c, C_CU + (s >> 3), 320, 0x300);
#pragma unroll
    for (int i = 0; i < 32; ++i) bx[i] = ld16c(c.ws, l16, WS_UX + ((s * 40 + ct) * 32 + i) * 1024);
    SB();
    for (int sp = 0; rfl(done[0]) < (unsigned)s && sp < (1 << 24); ++sp) __builtin_amdgcn_s_sleep(2);
    unsigned so = xrec_soff(c, s), su = um_soff(c, s) + ct * 16384, sn = xrec_soff(c, s + 1), sm = so + nmap(PCM_OFF);
    unsigned sx[4] = {so + nmap(PCX_OFF), so + nmap(PCX_OFF + 4096), so + nmap(PCX_OFF + 8192), so + nmap(PCX_OFF + 12288)}, su2 = su + 4096;
    asm volatile("" : "+s"(sm), "+s"(su), "+s"(sn), "+s"(su2), "+s"(sx[0]), "+s"(sx[1]), "+s"(sx[2]), "+s"(sx[3]));
    {
      int ta1 = (int)rfl(done[1]), ae = (int)rfl(done[2]), pe = (int)rfl(done[3]);
      const int ts1 = s > 0 ? last_pub(c, s - 1, TS_OFF, 32, l16) : -1;
      if (ts1 >= 0 && ta1 >= 0) ae = min(ts1 - ta1, ae + LEAK);
      if (ta1 >= 0 && pe < 400) wait_until(c, ta1 + pe + PRE_M);
      const int ta = wait_arr(c, s, l16);
      if (ta >= 0 && ta1 >= 0) pe = min(ta - ta1, pe + LEAK);
      done[1] = (unsigned)ta; done[2] = (unsigned)ae; done[3] = (unsigned)pe;
      if (ta >= 0 && ae < 300) wait_until(c, ta + ae + VIS_M);
    }
    STAMP(c, 64 + ct, s, 0);
    unsigned res;
    POLL(c, bud, 0x500 + s, {
      u32x4 ax[16]; u32x2 am[8];
      OPQ(l16); OPQ(vm); __builtin_assume(l16 < 1024u); __builtin_assume(vm < 1024u);
      if (!UM_LAST) { _Pragma("unroll") for (int i = 0; i < 8; ++i) bm[i] = ld16c(c.ws, l16 + (i & 3) * 1024, i < 4 ? su : su2); }
      SB();
      _Pragma("unroll") for (int i = 0; i < 16; ++i) { ax[i] = ld16c(c.ws, l16 + (i & 3) * 1024, sx[i >> 2]); SB(); }
      _Pragma("unroll") for (int i = 0; i < 8; ++i) { am[i] = ld8c(c.ws, vm + (i >> 1) * 1024 + (i & 1) * 128, sm); SB(); }
      if (UM_LAST) { _Pragma("unroll") for (int i = 0; i < 8; ++i) { bm[i] = ld16c(c.ws, l16 + (i & 3) * 1024, i < 4 ? su : su2); SB(); } }
      f32x4 acc = {};
      SB();
      _Pragma("unroll") for (int i = 0; i < 16; ++i) acc = SMF(__builtin_bit_cast(bf16x8, ax[i]), cat8(__builtin_bit_cast(bf16x8, bx[2 * i]), __builtin_bit_cast(bf16x8, bx[2 * i + 1])), acc, idx);
      _Pragma("unroll") for (int i = 0; i < 4; ++i) { u32x4 a4 = {am[2 * i][0], am[2 * i][1], am[2 * i + 1][0], am[2 * i + 1][1]};
        acc = SMF(__builtin_bit_cast(bf16x8, a4), cat8(__builtin_bit_cast(bf16x8, bm[2 * i]), __builtin_bit_cast(bf16x8, bm[2 * i + 1])), acc, idx); }
      const f32x2 hs = hsum(acc); const float s0 = hs[0]; const float s1 = hs[1];
      const bool odd = lane & 1;
      const float t = __uint_as_float(dpp_x1(__float_as_uint(odd ? s0 : s1)));
      res = pk2s(odd ? t : s0, odd ? s1 : t); OPQ(res);
      ok = __all(s0 == s0 && s1 == s1);
      if (!ok) { unsigned m = 0;
        _Pragma("unroll") for (int i = 0; i < 16; ++i) m = umax4(m, ax[i]);
        _Pragma("unroll") for (int i = 0; i < 8; ++i) m = umax4(umax3(m, am[i][0], am[i][1]), bm[i]);
        ok = __all(m != SENT); } });
    st4c(res, c.ws, vpub, sn);
    STAMP(c, 64 + ct, s, 2);
    st4c((unsigned)now(c), c.ws, 0, far_soff(c, s + 1) + TM_OFF + ct * 4);
    done[0] = (unsigned)(s + 1);
    if (TOUCH && s + 9 < NS_) st4c(SENT, c.ws, vpub, xrec_soff(c, s + 9));
    FAIL0(s); STAMP_FLUSH(true, s, 5);
  }
  FAILS(w);
}

template <bool SC> DI void xload(u32x4& d, rsrc_t r, unsigned v, unsigned s) {
  if (SC) asm volatile("buffer_load_dwordx4 %0, %1, %2, %3 offen sc1" : "=v"(d) : "v"(v), "s"(r), "s"(s) : "memory");
  else asm volatile("buffer_load_dwordx4 %0, %1, %2, %3 offen" : "=v"(d) : "v"(v), "s"(r), "s"(s) : "memory");
}
#define XPIN(a, b) asm volatile("" : "+v"(a), "+v"(b))
#define XWIN 4
constexpr int WIN = XWIN;
constexpr unsigned STG = 24 * KB;

struct WStream { rsrc_t r; unsigned v[3]; unsigned s0, step; };

struct Ring { unsigned rb[4], cur[4], slot; };
DI void ring_next(Ring& r) {
  r.slot = r.slot == 2 * STG ? 0u : r.slot + STG;
#pragma unroll
  for (int j = 0; j < 4; ++j) { r.cur[j] = r.rb[j] + r.slot; OPQ(r.cur[j]); }
}
template <bool PI, int NST, bool XSC, int MODE>
DI void proj_stage(const Ctx& c, f32x4 (&acc)[12][2], u32x4 (&X)[2][2], bf16x8 (&A)[WIN], Ring& rg, int t, const WStream& ws, unsigned xs, unsigned xts, const WStream& nx, bool has_next, unsigned l16) {
  auto xl = [&](int ks) { xload<XSC>(X[ks][0], c.ws, l16, xs + (2 * t + 4 + ks) * 1024); xload<XSC>(X[ks][1], c.ws, l16, xs + (xts + 2 * t + 4 + ks) * 1024); };
#pragma unroll
  for (int q = 0; q < 24; ++q) {
    const int ks = q / 12, T = q % 12;
    if (q == 0) XPIN(X[0][0], X[0][1]);
    if (q == 12) {
      if (MODE == 1) { if (has_next) WAIT_V(7); else WAIT_V(4); } else if (MODE == 2) { if (has_next) WAIT_V(3); else WAIT_V(0); } else WAIT_V(7);
      XPIN(X[1][0], X[1][1]);
      if (MODE == 0) xl(0);
    }
    const bf16x8 a = A[q % WIN];
    acc[T][0] = MFMA(a, __builtin_bit_cast(bf16x8, X[ks][0]), acc[T][0]);
    acc[T][1] = MFMA(a, __builtin_bit_cast(bf16x8, X[ks][1]), acc[T][1]);
    XPIN(acc[T][0], acc[T][1]);
    SB();
    if (q == 23 && MODE == 0) xl(1);
    if (q + WIN == 24 && MODE != 2) {
      if (MODE == 1) { if (has_next) WAIT_V(5); else WAIT_V(2); } else WAIT_V(7);
      if (XG != 3) LBAR();
      if (MODE == 0 && t + 3 < NST) stage_load(ws.r, ws.v, ws.s0 + (t + 3) * ws.step, rg.slot, c.wave);
      else if (has_next) stage_load(nx.r, nx.v, nx.s0 + (MODE == 1 ? nx.step : 0u), rg.slot, c.wave);
      ring_next(rg);
      SB();
    }
    if (q + WIN < 24 || MODE != 2) {
      const int f = (q + WIN) % 24;
      if (XG == 1 && (q & 1)) A[q % WIN] = A[(q + WIN - 1) % WIN]; else if (XG == 2) A[q % WIN] = ldsr16(rg.cur[0] + f * 1024); else
      A[q % WIN] = pfmt_frag<PI>(rg.cur[(f % 12) & 3] + ((f % 12) >> 2) * 128, f / 12);
      SB();
    }
  }
}
template <bool PI, int NST, bool XSC> DI void proj(const Ctx& c, f32x4 (&acc)[12][2], const WStream& ws, unsigned xs, unsigned xts, const WStream& nx, bool has_next, unsigned l16) {
  const int w = c.wave;
  Ring rg;
#pragma unroll
  for (int j = 0; j < 4; ++j) rg.rb[j] = XG == 2 ? l16 : pfmt_base<PI>(c.lane, j);
  u32x4 X0[2][2], X1[2][2];
  stage_load(ws.r, ws.v, ws.s0, 0, w);
#pragma unroll
  for (int i = 0; i < 4; ++i) xload<XSC>(X0[i >> 1][i & 1], c.ws, l16, xs + ((i & 1) * xts + (i >> 1)) * 1024);
  stage_load(ws.r, ws.v, ws.s0 + ws.step, STG, w);
#pragma unroll
  for (int i = 0; i < 4; ++i) xload<XSC>(X1[i >> 1][i & 1], c.ws, l16, xs + ((i & 1) * xts + 2 + (i >> 1)) * 1024);
  stage_load(ws.r, ws.v, ws.s0 + 2 * ws.step, 2 * STG, w);
#pragma unroll
  for (int T = 0; T < 12; ++T) { acc[T][0] = f32x4{}; acc[T][1] = f32x4{}; }
  bf16x8 A[WIN];
  rg.slot = 2 * STG; ring_next(rg);
  WAIT_V(12);
  LBAR();
#pragma unroll
  for (int f = 0; f < WIN; ++f) A[f] = pfmt_frag<PI>(rg.cur[f & 3] + (f >> 2) * 128, 0);
  SB();
#pragma nounroll
  for (int t = 0; t < NST - 2; t += 2) {
    proj_stage<PI, NST, XSC, 0>(c, acc, X0, A, rg, t, ws, xs, xts, nx, has_next, l16);
    proj_stage<PI, NST, XSC, 0>(c, acc, X1, A, rg, t + 1, ws, xs, xts, nx, has_next, l16);
  }
  proj_stage<PI, NST, XSC, 1>(c, acc, X0, A, rg, NST - 2, ws, xs, xts, nx, has_next, l16);
  proj_stage<PI, NST, XSC, 2>(c, acc, X1, A, rg, NST - 1, ws, xs, xts, nx, has_next, l16);
}

template <bool LAST> DI void u_stage(const Ctx& c, const bf16x8 (&VF)[6][2], f32x4 (&o)[2][2], bf16x8 (&B)[WIN], Ring& rg, int t, const WStream& ws, unsigned uo, unsigned l16, int nu) {
#pragma unroll
  for (int q = 0; q < 24; ++q) {
    const int m = q % 6, C = q / 6;
    const bf16x8 b = B[q % WIN];
    o[C & 1][0] = MFMA(VF[m][0], b, m == 0 ? f32x4{} : o[C & 1][0]);
    o[C & 1][1] = MFMA(VF[m][1], b, m == 0 ? f32x4{} : o[C & 1][1]);
    XPIN(o[C & 1][0], o[C & 1][1]);
    SB();
    if (m == 1 && (C > 0 || t > 0)) { st16c(__builtin_bit_cast(u32x4, pack8(o[(C + 1) & 1][0], o[(C + 1) & 1][1])), c.ws, l16, uo + (4 * t + C - 1) * 32768); SB(); }
    if (q + WIN == 24 && !LAST) {
      WAIT_V(6);
      LBAR();
      if (t + 3 < nu) stage_load(ws.r, ws.v, ws.s0 + (t + 3) * ws.step, rg.slot, c.wave);
      ring_next(rg);
      SB();
    }
    if (q + WIN < 24 || !LAST) { const int f = (q + WIN) % 24; B[q % WIN] = ufmt_frag(rg.cur[f / 6], f % 6); SB(); }
  }
}
DI void uphase(const Ctx& c, const f32x4 (&acc)[12][2], const WStream& ws, unsigned uo, unsigned l16, int nu) {
  Ring rg;
#pragma unroll
  for (int j = 0; j < 4; ++j) rg.rb[j] = ufmt_base(c.lane, j);
  bf16x8 B[WIN];
  rg.slot = 0; ring_next(rg);
  WAIT_V(3);
  LBAR();
  stage_load(ws.r, ws.v, ws.s0 + 2 * ws.step, 0, c.wave);
#pragma unroll
  for (int f = 0; f < WIN; ++f) B[f] = ufmt_frag(rg.cur[f / 6], f % 6);
  SB();
  bf16x8 VF[6][2];
#pragma unroll
  for (int m = 0; m < 6; ++m) { VF[m][0] = pack8(acc[2 * m][0], acc[2 * m + 1][0]); VF[m][1] = pack8(acc[2 * m][1], acc[2 * m + 1][1]); XPIN(VF[m][0], VF[m][1]); }
  SB();
  f32x4 o[2][2] = {};
#pragma nounroll
  for (int t = 0; t < nu - 1; ++t) u_stage<false>(c, VF, o, B, rg, t, ws, uo, l16, nu);
  u_stage<true>(c, VF, o, B, rg, nu - 1, ws, uo, l16, nu);
  st16c(__builtin_bit_cast(u32x4, pack8(o[1][0], o[1][1])), c.ws, l16, uo + (4 * nu - 1) * 32768);
}

DI void signal_wg(const Ctx& c, int word, unsigned inc = 1) {
  WAIT_V(0);
  BAR();
  if (c.wave == 0 && c.lane == 0) __hip_atomic_fetch_add(cw(c, word), inc, RLX_AGENT);
}

DI void attn_tail(const Ctx& c, const f32x4 (&acc)[12][2], int s, int h, unsigned l16) {
  const int lane = c.lane, g = lane >> 4, tok = lane & 15;
  bf16x8 QF[6][2];
#pragma unroll
  for (int m = 0; m < 6; ++m) { QF[m][0] = pack8(acc[2 * m][0], acc[2 * m + 1][0]); QF[m][1] = pack8(acc[2 * m][1], acc[2 * m + 1][1]); }
  wait_ge(c, C_FK + (s >> 3) * 32 + h, 1, 0x600);
  f32x4 x00 = {}, x10 = {}, x11 = {}, m0 = {}, m1 = {};
  {
    u32x4 kx[12];
#pragma unroll
    for (int i = 0; i < 12; ++i) kx[i] = ld16c(c.ws, l16, WS_KX + ((s * 32 + h) * 12 + i) * 1024);
    SB();
#pragma unroll
    for (int m = 0; m < 6; ++m) {
      x00 = MFMA(__builtin_bit_cast(bf16x8, kx[m]), QF[m][0], x00);
      x10 = MFMA(__builtin_bit_cast(bf16x8, kx[m]), QF[m][1], x10);
      x11 = MFMA(__builtin_bit_cast(bf16x8, kx[6 + m]), QF[m][1], x11);
    }
  }
  wait_ge(c, C_PS + h, s + 1, 0x601, true);
  {
    u32x4 km[6];
#pragma unroll
    for (int i = 0; i < 6; ++i) km[i] = ld16c(c.ws, l16, WS_KM + ((s * 32 + h) * 6 + i) * 1024);
    SB();
#pragma unroll
    for (int m = 0; m < 6; ++m) { m0 = MFMA(__builtin_bit_cast(bf16x8, km[m]), QF[m][0], m0); m1 = MFMA(__builtin_bit_cast(bf16x8, km[m]), QF[m][1], m1); }
  }
  const float sc = 0.07216878364870322f * 1.4426950408889634f, NEG = -1.0e9f * 1.4426950408889634f;
#pragma unroll
  for (int a = 0; a < 2; ++a) {
    f32x4 lo = a ? x10 : x00, hi = x11, mm = a ? m1 : m0;
#pragma unroll
    for (int i = 0; i < 4; ++i) {
      mm[i] *= sc;
      const bool keep = 4 * g + i <= tok;
      if (a == 0) { lo[i] = keep ? lo[i] * sc : NEG; hi[i] = NEG; } else { lo[i] *= sc; hi[i] = keep ? hi[i] * sc : NEG; }
    }
    float mx = fmaxf(fmaxf(fmaxf(lo[0], lo[1]), fmaxf(lo[2], lo[3])), fmaxf(mm[0], mm[1]));
    mx = fmaxf(mx, fmaxf(fmaxf(hi[0], hi[1]), fmaxf(hi[2], hi[3])));
    mx = x32max(x16max(mx));
    mm[0] = __builtin_amdgcn_exp2f(mm[0] - mx); mm[1] = __builtin_amdgcn_exp2f(mm[1] - mx);
    float l = mm[0] + mm[1];
#pragma unroll
    for (int i = 0; i < 4; ++i) { lo[i] = __builtin_amdgcn_exp2f(lo[i] - mx); hi[i] = __builtin_amdgcn_exp2f(hi[i] - mx); l += lo[i] + hi[i]; }
    l = x32sum(x16sum(l));
    const float inv = 1.0f / l;
    const unsigned po = WS_PX + (s * 2 + a) * 40960;
    st16c(__builtin_bit_cast(u32x4, pack8(lo * inv, hi * inv)), c.ws, l16, po + h * 1024);
    st4c(pk2(mm[0] * inv, mm[1] * inv), c.ws, tok * 16 + g * 4, po + 32768 + h * 256);
  }
  WAIT_V(0);
  if (lane == 0) __hip_atomic_fetch_add(cw(c, C_CP + s), 1u, RLX_AGENT);
}

DI void fin_item(const Params& p, const Ctx& c, int s, int part, unsigned l16) {
  const int lane = c.lane, w = c.wave, g = lane >> 4, tok = lane & 15;
  auto wait_words = [&](bool all) {
    const int word = lane < 32 ? C_PV + lane : lane == 32 ? C_CP + s : lane == 33 ? C_CU + (s >> 3) : C_CG + (s >> 3);
    const unsigned want = lane > 33 ? 4u : lane == 33 ? 320u : !all ? 0u : lane == 32 ? 32u : (unsigned)s + 1u;
    for (unsigned spins = 0;;) {
      const unsigned v = __hip_atomic_load(cw(c, word), RLX_AGENT);
      if (__all(v >= want)) break;
      if (__all(lane >= 32 || v + 1 >= want)) __builtin_amdgcn_s_sleep(2); else __builtin_amdgcn_s_sleep(32);
      if (spin_fail(c, spins, 0x700)) break;
    }
  };
  if (w == 0) wait_words(false);
  LBAR();
  const int ct = part * 8 + w;
  u32x4 u[40];
#pragma unroll
  for (int i = 0; i < 32; ++i) { u[i] = ld16c(XF == 1 ? mk_rsrc(p.ws, 0) : c.ws, l16, WS_UX + ((s * 40 + ct) * 32 + i) * 1024); if (FIN_THR && i >= FIN_THR) OPQ(u[i - FIN_THR]); }
  f32x4 gt[2]; u32x2 xv[2];
#pragma unroll
  for (int a = 0; a < 2; ++a) {
    gt[a] = __builtin_bit_cast(f32x4, ld16c(c.ws, l16, WS_G + ((s * 2 + a) * 40 + ct) * 1024));
    xv[a] = *(const u32x2*)(p.x + (size_t)(s * 32 + a * 16 + tok) * D_ + ct * 16 + g * 4);
  }
  SB();
  if (w == 0) wait_words(true);
  BAR();
  {
    u32x4 pv[10];
    const unsigned su = um_soff(c, s) + ct * 16384;
#pragma unroll
    for (int i = 0; i < 10; ++i) pv[i] = ld16c(XF == 3 ? mk_rsrc(p.ws, 0) : c.ws, l16, WS_PX + s * 81920 + (w * 10 + i) * 1024);
#pragma unroll
    for (int i = 0; i < 8; ++i) u[32 + i] = ld16c(XF == 3 ? mk_rsrc(p.ws, 0) : c.ws, l16, su + i * 1024);
    SB();
#pragma unroll
    for (int i = 0; i < 10; ++i) *(LAS u32x4*)(l16 + (w * 10 + i) * 1024) = pv[i];
  }
  LBAR();
  unsigned pa = l16, pb = l16 + 40960; OPQ(pa); OPQ(pb);
  f32x4 o0 = {}, o1 = {};
  bf16x8 b0[4], b1[4];
#pragma unroll
  for (int i = 0; i < 3; ++i) { b0[i] = ldsr16(pa + i * 1024); b1[i] = ldsr16(pb + i * 1024); }
#pragma unroll
  for (int i = 0; i < 40; ++i) {
    if (i + 3 < 40) { b0[(i + 3) & 3] = ldsr16(pa + (i + 3) * 1024); b1[(i + 3) & 3] = ldsr16(pb + (i + 3) * 1024); }
    SB();
    o0 = MFMA(__builtin_bit_cast(bf16x8, u[i]), b0[i & 3], o0); o1 = MFMA(__builtin_bit_cast(bf16x8, u[i]), b1[i & 3], o1);
    XPIN(o0, o1); SB();
  }
#pragma unroll
  for (int a = 0; a < 2; ++a) {
    const f32x4 o = a ? o1 : o0;
    u32x2 r = {pk2(b2f(xv[a][0] & 0xffffu) + o[0] * gt[a][0], __uint_as_float(xv[a][0] & 0xffff0000u) + o[1] * gt[a][1]),
               pk2(b2f(xv[a][1] & 0xffffu) + o[2] * gt[a][2], __uint_as_float(xv[a][1] & 0xffff0000u) + o[3] * gt[a][3])};
    if (XF != 2 || r[0] == 0x12345u) *(u32x2*)(p.out + (size_t)(s * 32 + a * 16 + tok) * D_ + ct * 16 + g * 4) = r;
  }
}

enum { I_VU = 0, I_K = 1, I_F1 = 2, I_Q = 3, I_F2 = 4, I_FIN = 5 };
struct ItemTab { unsigned v[1280]; int n; };
constexpr unsigned item(int type, int rb, int h, int u0 = 0, int nu = 10) { return (unsigned)(type | rb << 3 | h << 6 | u0 << 12 | nu << 16); }
constexpr ItemTab make_items() {
  ItemTab t = {};
  int n = 0;
  auto grp = [&](int type, int rb, int cnt) { for (int h = 0; h < cnt; ++h) t.v[n++] = item(type, rb, h); };
  auto A = [&](int rb) { grp(I_VU, rb, 32); grp(I_K, rb, 32); };
  auto mix = [&](int q, int f0, int nf) {
    int fi = 0;
    for (int h = 0; h < 32; ++h) { t.v[n++] = item(I_Q, q, h); for (; fi < (h + 1) * nf * 40 / 32; ++fi) t.v[n++] = item(I_FIN, f0 + fi / 40, fi % 40); }
  };
  auto mixA = [&](int rb, int f) {
    int fi = 0;
    for (int i = 0; i < 64; ++i) { t.v[n++] = item(i < 32 ? I_VU : I_K, rb, i & 31); for (; fi < (i + 1) * 40 / 64; ++fi) t.v[n++] = item(I_FIN, f, fi); }
  };
  for (int h = 0; h < 32; ++h) { t.v[n++] = item(I_VU, 0, h, 0, 5); t.v[n++] = item(I_VU, 0, h, 5, 5); }
  grp(I_K, 0, 32); grp(I_VU, 1, 32); grp(I_F1, 0, 8); grp(I_F1, 1, 8); grp(I_K, 1, 32);
  grp(I_F2, 0, 4); grp(I_F2, 1, 4); A(2); grp(I_F1, 2, 8); grp(I_F1, 3, 8);
  grp(I_Q, 0, 32); grp(I_F2, 2, 4); grp(I_F2, 3, 4); A(3);
  for (int rb = 4; rb < 8; ++rb) grp(I_F1, rb, 8);
  A(4);
  for (int rb = 4; rb < 8; ++rb) grp(I_F2, rb, 4);
  grp(I_Q, 1, 32); mixA(5, 0); grp(I_Q, 2, 32); mixA(6, 1); grp(I_Q, 3, 32); mixA(7, 2);
  mix(4, 3, 1);
  for (int rb = 5; rb < 8; ++rb) { grp(I_Q, rb, 32); grp(I_FIN, rb - 1, 40); }
  grp(I_FIN, 7, 40);
  t.n = n;
  return t;
}
__device__ constexpr ItemTab ITEMS = make_items();
constexpr int N_ITEMS = make_items().n;
static_assert(N_ITEMS == 32 + 256 * 3 + 64 + 32 + 320, "item count");
DI int next_item(const Ctx& c) {
  LAS unsigned* tk = (LAS unsigned*)(LDS_BYTES - 16);
  LBAR();
  if (c.wave == 0 && c.lane == 0) *tk = __hip_atomic_fetch_add(cw(c, C_Q), 1u, RLX_AGENT);
  LBAR();
  return (int)rfl(*tk);
}
DI void bulk(const Params& p, const Ctx& c, int lo, int hi) {
  const int lane = c.lane, w = c.wave, g = lane >> 4;
  unsigned l16 = lane * 16; OPQ(l16); l16 &= 0x3F0u;
#define BSTAMP(k, v) do {} while (0)
  for (;;) {
    const int it = lo + next_item(c);
    if (it >= hi) break;
    const unsigned code = ITEMS.v[it];
    const int type = code & 7, rb = (code >> 3) & 7, h = (code >> 6) & 63, u0 = (code >> 12) & 15, nu = code >> 16;
    const int s = rb * 8 + w;
    if (type == 5) { fin_item(p, c, rb * 8 + h / 5, h % 5, l16); continue; }
    f32x4 acc[12][2];
    if (type == 4) {
      if (w == 0) wait_ge(c, C_F1 + rb, 8, 0x800);
      LBAR();
      const int n0 = h < 3 ? h * 192 : 448;
      WStream ws; ws.r = mk_rsrc(p.w2, F_ * D_ * 2); pfmt_voff<true>(ws.v, w, lane, 1280); ws.s0 = n0 * 2; ws.step = 64 * 1280;
      proj<true, 24, true>(c, acc, ws, WS_HF + s * 2 * 48 * 1024, 48, ws, false, l16);
#pragma unroll
      for (int T = 0; T < 12; ++T) {
        const u32x2 bv = *(const u32x2*)(p.b2 + n0 + T * 16 + g * 4);
        const f32x4 bb = {b2f(bv[0] & 0xffffu), __uint_as_float(bv[0] & 0xffff0000u), b2f(bv[1] & 0xffffu), __uint_as_float(bv[1] & 0xffff0000u)};
#pragma unroll
        for (int a = 0; a < 2; ++a) {
          f32x4 v = acc[T][a] + bb;
#pragma unroll
          for (int i = 0; i < 4; ++i) v[i] = v[i] / (1.0f + __expf(-v[i]));
          if (h < 3 || T >= 8) st16c(__builtin_bit_cast(u32x4, v), c.ws, l16, WS_G + ((s * 2 + a) * 40 + n0 / 16 + T) * 1024);
        }
      }
      signal_wg(c, C_CG + rb);
      continue;
    }
    WStream ws, nx;
    const unsigned pitch = type == 2 ? 3072 : 12288;
    ws.r = mk_rsrc(type == 0 ? p.Wv : type == 1 ? p.Wk : type == 2 ? p.w1 : p.Wq, XNOW * D_ * pitch);
    pfmt_voff<false>(ws.v, w, lane, pitch); ws.s0 = h * 384; ws.step = 64 * pitch;
    nx.r = mk_rsrc(p.Wo, XNOW * D_ * H_ * E_ * 2); ufmt_voff(nx.v, w, lane); nx.s0 = h * 192 * 1280 + u0 * 128; nx.step = 128;
    Ctx c0 = c; c0.ws = mk_rsrc(p.ws, 0);
    proj<false, 10, false>(XNOX ? c0 : c, acc, ws, WS_XN + s * 2 * 20 * 1024, 20, nx, type == 0, l16);
    if (type == 0) {
      uphase(XNOS ? c0 : c, acc, nx, WS_UX + ((s * 40 + u0 * 4) * 32 + h) * 1024, l16, nu);
      signal_wg(c, C_CU + rb, nu);
    } else if (type == 1) {
#pragma unroll
      for (int a = 0; a < 2; ++a)
#pragma unroll
        for (int m = 0; m < 6; ++m) st16c(__builtin_bit_cast(u32x4, pack8(acc[2 * m][a], acc[2 * m + 1][a])), c.ws, l16, WS_KX + ((s * 32 + h) * 12 + a * 6 + m) * 1024);
      signal_wg(c, C_FK + rb * 32 + h);
    } else if (type == 2) {
#pragma unroll
      for (int T = 0; T < 12; ++T) {
        const u32x2 bv = *(const u32x2*)(p.b1 + h * 192 + T * 16 + g * 4);
        const f32x4 bb = {b2f(bv[0] & 0xffffu), __uint_as_float(bv[0] & 0xffff0000u), b2f(bv[1] & 0xffffu), __uint_as_float(bv[1] & 0xffff0000u)};
#pragma unroll
        for (int a = 0; a < 2; ++a) {
          f32x4 v = acc[T][a] + bb;
#pragma unroll
          for (int i = 0; i < 4; ++i) { const float r = v[i] > 0.f ? v[i] : 0.f; v[i] = r * r; }
          acc[T][a] = v;
        }
      }
#pragma unroll
      for (int a = 0; a < 2; ++a)
#pragma unroll
        for (int m = 0; m < 6; ++m) st16c(__builtin_bit_cast(u32x4, pack8(acc[2 * m][a], acc[2 * m + 1][a])), c.ws, l16, WS_HF + ((s * 2 + a) * 48 + h * 6 + m) * 1024);
      signal_wg(c, C_F1 + rb);
    } else {
      attn_tail(c, acc, s, h, l16);
    }
  }
}

DI void ctx_init(Ctx& c, const Params& p, char* smem) {
  char* base = ws_base(p.ws);
  c.ws = mk_rsrc(base, WS_END);
  c.ctl = (gu32*)(base + WS_CTL);
  c.lds = 0u;
  c.lane = threadIdx.x & 63; c.wave = rfl(threadIdx.x >> 6);
  unsigned pv = c.lane < 24 ? __hip_atomic_load(cw(c, C_PAR + c.lane), RLX_AGENT) : 0u;
  c.par = (unsigned)__ballot(pv != 0);
  c.t0 = rfl(__hip_atomic_load(cw(c, C_T0), RLX_AGENT));
}
DI int get_role(const Ctx& c, int nwg) {
  LAS unsigned* tk = (LAS unsigned*)(c.lds + LDS_BYTES - 16);
  if (threadIdx.x == 0) {
    int r;
    if (xcc_id() < 4) { r = (int)__hip_atomic_fetch_add(cw(c, C_TKA), 1u, RLX_AGENT); }
    else { r = nwg - 1 - (int)__hip_atomic_fetch_add(cw(c, C_TKB), 1u, RLX_AGENT); }
    *tk = (unsigned)r;
  }
  LBAR();
  int r = (int)rfl(*tk);
  LBAR();
  return r;
}

__global__ void __launch_bounds__(512, 2) k_main(Params p) {
  extern __shared__ __attribute__((aligned(16))) char smem[];
  Ctx c; ctx_init(c, p, smem);
  const int role = get_role(c, 256);
#define ROLE(call) do { asm volatile("v_mbcnt_lo_u32_b32 %0, -1, 0\n\tv_mbcnt_hi_u32_b32 %0, -1, %0" : "=v"(c.lane)); call; STAMP_DUMP(c, role); __builtin_amdgcn_endpgm(); } while (0)
  if (role < 32) { if (c.wave < 2) ROLE(s_wave<0>(p, c, role)); else if (c.wave == 2) ROLE(s_wave<1>(p, c, role)); else if (c.wave == 3) ROLE(s_wave<3>(p, c, role)); else ROLE(s_wave<2>(p, c, role)); }
  else if (role < 64) { if (c.wave < 4) ROLE(v_wave<true>(p, c, role - 32)); else ROLE(v_wave<false>(p, c, role - 32)); }
  else if (role < NCHAIN) ROLE(m_wave(p, c, role - 64));
  else if (XF == 4 && role < 128) return;
  else ROLE(bulk(p, c, 0, N_ITEMS));
}

__global__ void __launch_bounds__(256) k_prep(Params p) {
  __shared__ float red[2][4][16];
  char* base = ws_base(p.ws);
  const int b = blockIdx.x, tid = threadIdx.x, lane = tid & 63, w = tid >> 6;
  if (b < 128) {
    const int tok = lane & 15, g = lane >> 4;
    const u32x4* src = (const u32x4*)(p.x + (size_t)(b * 16 + tok) * D_ + w * 160 + g * 8);
    float v[40];
#pragma unroll
    for (int k = 0; k < 5; ++k) { u32x4 d = src[k * 4];
#pragma unroll
      for (int j = 0; j < 4; ++j) { v[k * 8 + 2 * j] = b2f(d[j] & 0xffffu); v[k * 8 + 2 * j + 1] = __uint_as_float(d[j] & 0xffff0000u); } }
    float s = 0.f;
#pragma unroll
    for (int i = 0; i < 40; ++i) s += v[i];
    s = x32sum(x16sum(s));
    if (lane < 16) red[0][w][lane] = s;
    __syncthreads();
    const float mean = (red[0][0][tok] + red[0][1][tok] + red[0][2][tok] + red[0][3][tok]) * (1.0f / D_);
    float q = 0.f;
#pragma unroll
    for (int i = 0; i < 40; ++i) { v[i] -= mean; q += v[i] * v[i]; }
    q = x32sum(x16sum(q));
    if (lane < 16) red[1][w][lane] = q;
    __syncthreads();
    const float var = (red[1][0][tok] + red[1][1][tok] + red[1][2][tok] + red[1][3][tok]) * (1.0f / D_);
    const float rs = 1.0f / sqrtf(var + 1e-5f);
    u32x4* dst = (u32x4*)(base + WS_XN + (size_t)(b * 20 + w * 5) * 1024) + lane;
#pragma unroll
    for (int k = 0; k < 5; ++k) { u32x4 d;
#pragma unroll
      for (int j = 0; j < 4; ++j) d[j] = pk2(v[k * 8 + 2 * j] * rs, v[k * 8 + 2 * j + 1] * rs);
      dst[k * 64] = d; }
  } else if (b < 152) {
    const int pg = b - 128;
    if (w == 0) {
      rsrc_t r = mk_rsrc(base, WS_END);
      const unsigned o = pg * (2 * MB) + (pg < 2 ? 31 * 65536 + 49152 : 1920 * KB);
      unsigned best[2] = {~0u, ~0u};
      for (int rep = 0; rep < 4; ++rep)
#pragma unroll
        for (int hh = 0; hh < 2; ++hh) {
          unsigned long long t0 = __builtin_amdgcn_s_memtime();
          st4c(SENT, r, (lane & 31) * 4, o + hh * 8192); WAIT_V(0);
          unsigned long long t1 = __builtin_amdgcn_s_memtime();
          best[hh] = min(best[hh], (unsigned)(t1 - t0));
        }
      if (lane == 0) ((unsigned*)(base + WS_CTL))[(C_PAR + pg) * 32] = (unsigned)(best[1] < best[0]) ^ (unsigned)(xcc_id() >= 4);
    }
  } else if (b == 152) {
    for (int i = tid; i < C_N; i += 256) if (i < C_PAR || i >= C_PAR + 24) ((unsigned*)(base + WS_CTL))[i * 32] = i == C_T0 ? (unsigned)__builtin_amdgcn_s_memrealtime() : 0u;
    for (int i = tid; i < 8 * 320; i += 256) {
      const int r = i / 320, cp = i % 320, ct = cp >> 3, q = cp & 7;
      unsigned v = ((const unsigned*)p.mem0)[r * 320 + cp]; v = v < 0xFFFFFFFEu ? v : 0xFFFFFFFEu;
      const unsigned o = nmap(ct * 256 + (4 * (r >> 1) + 2 * (q & 1) + (r & 1)) * 16 + (q >> 1) * 4);
      *(unsigned*)(base + WS_XREC + o) = v; *(unsigned*)(base + WS_XREC + o + 8192) = v;
    }
  } else {
    const u32x4 sv = {SENT, SENT, SENT, SENT};
    const int nb = gridDim.x - 153;
    constexpr unsigned n16 = (4 * MB + 1280 * KB) / 16;
    for (unsigned i = (b - 153) * 256 + tid; i < n16; i += nb * 256) {
      const unsigned o = i * 16;
      if (o < 32768 && (o >> 14) * 8192 + (o & 8191u) < MEM_BYTES) continue;
      *(u32x4*)(base + o) = sv;
    }
  }
}

extern "C" void kernel_launch(void* const* d_in, const int* in_sizes, int n_in, void* d_out, int out_size, void* d_ws, size_t ws_size, hipStream_t stream) {
  Params p;
  p.x = (const u16*)d_in[0]; p.mem0 = (const u16*)d_in[1]; p.Wq = (const u16*)d_in[2]; p.Wk = (const u16*)d_in[3]; p.Wv = (const u16*)d_in[4]; p.Wo = (const u16*)d_in[5];
  p.w1 = (const u16*)d_in[8]; p.b1 = (const u16*)d_in[9]; p.w2 = (const u16*)d_in[10]; p.b2 = (const u16*)d_in[11];
  p.out = (u16*)d_out; p.ws = (char*)d_ws;
  hipFuncSetAttribute((const void*)k_main, hipFuncAttributeMaxDynamicSharedMemorySize, LDS_BYTES);
  k_prep<<<256, 256, 0, stream>>>(p);
  k_main<<<256, 512, LDS_BYTES, stream>>>(p);
}
```

```cpp
#include <hip/hip_runtime.h>
#include <hip/hip_bf16.h>
#include <stdint.h>

#define DI __device__ __forceinline__
#define LAS __attribute__((address_space(3)))
#define GAS __attribute__((address_space(1)))
typedef unsigned short u16;
typedef __bf16 bf16x2 __attribute__((ext_vector_type(2)));
typedef __bf16 bf16x4 __attribute__((ext_vector_type(4)));
typedef __bf16 bf16x8 __attribute__((ext_vector_type(8)));
typedef __bf16 bf16x16 __attribute__((ext_vector_type(16)));
typedef float f32x2 __attribute__((ext_vector_type(2)));
typedef float f32x4 __attribute__((ext_vector_type(4)));
typedef unsigned u32x2 __attribute__((ext_vector_type(2)));
typedef unsigned u32x4 __attribute__((ext_vector_type(4)));
typedef __amdgpu_buffer_rsrc_t rsrc_t;
typedef GAS unsigned gu32;
#define RLX_AGENT __ATOMIC_RELAXED, __HIP_MEMORY_SCOPE_AGENT

constexpr int T_ = 2048, D_ = 640, H_ = 32, E_ = 192, F_ = 1536, NS_ = 64;
constexpr unsigned MB = 1u << 20, KB = 1u << 10;
constexpr unsigned N_XREC = 0;
constexpr unsigned N_UM = 2 * MB;
constexpr unsigned N_KM = 24 * MB;
constexpr unsigned N_TP = 123 * MB;
constexpr unsigned F_UX = 0;
constexpr unsigned F_KX = 80 * MB;
constexpr unsigned F_HF = 104 * MB;
constexpr unsigned F_G = 110 * MB;
constexpr unsigned F_PX = 115 * MB;
constexpr unsigned F_XN = 120 * MB;
constexpr unsigned WS_CTL = 250 * MB;
constexpr unsigned WS_DBG = 251 * MB;
constexpr unsigned WS_END = 254 * MB;
constexpr int NPROBE = 31;
constexpr unsigned MEM_BYTES = 10240, PCM_OFF = 12288, PCX_OFF = 16384;
constexpr unsigned TA_OFF = 10240, TM_OFF = 0, TS_OFF = 512;
#define VIS_S 8
#define VIS_V 8
#define VIS_M 8
#define RST_DLY 24
#define UM_LAST 1
#define PF1 8
#define PROBE_REPS 3
#define UXROT 4
#define FIN_THR 2
#define KXN 1
#define PFD 1
#define ZE0 165
#define AE0 90
#define PE0 215
#define FIN_LEAD 5
#define LEAK 2
#define PRE_M 40
constexpr int C_TKA = 0, C_TKB = 1, C_ABORT = 2, C_Q = 3, C_T0 = 4, C_FK = 32  , C_CU = 288  , C_PS = 296  , C_PV = 328  ,
              C_F1 = 360  , C_CG = 368  , C_CP = 376  , C_N = 440, C_PAR = 440  ;
constexpr unsigned SENT = 0xFFFFFFFFu;
constexpr unsigned SPIN_CAP = 1u << 18;
constexpr int LDS_BYTES = 160 * 1024;
constexpr int NCHAIN = 104;

struct Params {
  const u16 *x, *mem0, *Wq, *Wk, *Wv, *Wo, *w1, *b1, *w2, *b2;
  u16* out; char* ws;
};

DI rsrc_t mk_rsrc(const void* p, unsigned bytes) { return __builtin_amdgcn_make_buffer_rsrc((void*)p, (short)0, (int)bytes, 0x00020000); }
DI char* ws_base(char* ws) { return (char*)(((uintptr_t)ws + (2 * MB - 1)) & ~(uintptr_t)(2 * MB - 1)); }
DI unsigned rfl(unsigned v) { return __builtin_amdgcn_readfirstlane(v); }
DI unsigned xcc_id() { return (unsigned)__builtin_amdgcn_s_getreg(20 | (3 << 11)) & 0xfu; }
DI unsigned nmap(unsigned o) { return ((o >> 13) << 14) | (o & 8191u); }
DI unsigned pk2(float a, float b) { f32x2 f = {a, b}; return __builtin_bit_cast(unsigned, __builtin_convertvector(f, bf16x2)); }
DI unsigned pk2s(float a, float b) { unsigned v = pk2(a, b); return v < 0xFFFFFFFEu ? v : 0xFFFFFFFEu; }
DI float b2f(unsigned v) { return __uint_as_float(v << 16); }
DI bf16x8 pack8(f32x4 a, f32x4 b) { u32x4 r = {pk2(a[0], a[1]), pk2(a[2], a[3]), pk2(b[0], b[1]), pk2(b[2], b[3])}; return __builtin_bit_cast(bf16x8, r); }
#define WAIT_V(n) asm volatile("s_waitcnt vmcnt(" #n ")" ::: "memory")
#define WAIT_L0() asm volatile("s_waitcnt lgkmcnt(0)" ::: "memory")
#define BAR() do { asm volatile("" ::: "memory"); __builtin_amdgcn_s_barrier(); asm volatile("" ::: "memory"); } while (0)
#define LBAR() do { WAIT_L0(); BAR(); } while (0)
#define MFMA(a, b, c) __builtin_amdgcn_mfma_f32_16x16x32_bf16((a), (b), (c), 0, 0, 0)
#define SMF(a, b, c, i) __builtin_amdgcn_smfmac_f32_16x16x64_bf16((a), (b), (c), (i), 0, 0)

DI void glds(rsrc_t r, unsigned voff, unsigned soff, unsigned ldsdst) {
  asm volatile("s_mov_b32 m0, %0\n\ts_nop 0\n\tbuffer_load_dwordx4 %1, %2, %3 offen lds" ::"s"(ldsdst), "v"(voff), "s"(r), "s"(soff) : "memory");
}
DI bf16x4 trd(unsigned a) { return __builtin_amdgcn_ds_read_tr16_b64_v4bf16((LAS bf16x4*)a); }
DI bf16x8 cat4(bf16x4 a, bf16x4 b) { return __builtin_shufflevector(a, b, 0, 1, 2, 3, 4, 5, 6, 7); }
DI bf16x16 cat8(bf16x8 a, bf16x8 b) { return __builtin_shufflevector(a, b, 0, 1, 2, 3, 4, 5, 6, 7, 8, 9, 10, 11, 12, 13, 14, 15); }
DI bf16x8 ldsr16(unsigned a) { return *(LAS bf16x8*)a; }
DI u32x4 ld16c(rsrc_t r, unsigned v, unsigned s) { return __builtin_amdgcn_raw_buffer_load_b128(r, v, s, 16); }
DI u32x2 ld8c(rsrc_t r, unsigned v, unsigned s) { return __builtin_amdgcn_raw_buffer_load_b64(r, v, s, 16); }
DI unsigned ld4c(rsrc_t r, unsigned v, unsigned s) { return __builtin_amdgcn_raw_buffer_load_b32(r, v, s, 16); }
DI void st16c(u32x4 d, rsrc_t r, unsigned v, unsigned s) { __builtin_amdgcn_raw_buffer_store_b128(d, r, v, s, 16); asm volatile("s_nop 1" ::"v"(d) : "memory"); }
DI void st8c(u32x2 d, rsrc_t r, unsigned v, unsigned s) { __builtin_amdgcn_raw_buffer_store_b64(d, r, v, s, 16); }
DI void st4c(unsigned d, rsrc_t r, unsigned v, unsigned s) { __builtin_amdgcn_raw_buffer_store_b32(d, r, v, s, 16); }
DI unsigned umax3(unsigned a, unsigned b, unsigned c) { return max(max(a, b), c); }
DI unsigned umax4(unsigned m, u32x4 v) { return umax3(umax3(m, v[0], v[1]), v[2], v[3]); }
DI float swp32_sum(float a, float b) {
  u32x2 r = __builtin_amdgcn_permlane32_swap(__float_as_uint(a), __float_as_uint(b), false, false);
  return __uint_as_float(r[0]) + __uint_as_float(r[1]);
}
DI float x16max(float v) { u32x2 r = __builtin_amdgcn_permlane16_swap(__float_as_uint(v), __float_as_uint(v), false, false); return fmaxf(__uint_as_float(r[0]), __uint_as_float(r[1])); }
DI float x32max(float v) { u32x2 r = __builtin_amdgcn_permlane32_swap(__float_as_uint(v), __float_as_uint(v), false, false); return fmaxf(__uint_as_float(r[0]), __uint_as_float(r[1])); }
DI float x16sum(float v) { u32x2 r = __builtin_amdgcn_permlane16_swap(__float_as_uint(v), __float_as_uint(v), false, false); return __uint_as_float(r[0]) + __uint_as_float(r[1]); }
DI float x32sum(float v) { u32x2 r = __builtin_amdgcn_permlane32_swap(__float_as_uint(v), __float_as_uint(v), false, false); return __uint_as_float(r[0]) + __uint_as_float(r[1]); }
DI unsigned dpp_shr8(unsigned keep, unsigned v) { return __builtin_amdgcn_update_dpp(keep, v, 0x118, 0xf, 0xf, false); }
DI unsigned dpp_x1(unsigned v) { return __builtin_amdgcn_update_dpp(v, v, 0xB1, 0xf, 0xf, false); }

struct Ctx {
  rsrc_t ws; gu32* ctl; unsigned lds; int lane, wave; unsigned par, t0;
};
DI int now(const Ctx& c) { return (int)(((unsigned)__builtin_amdgcn_s_memrealtime() - c.t0) & 0x7FFFFFFFu); }
DI void wait_until(const Ctx& c, int t) { for (int i = 0; i < 4096 && now(c) < t; ++i) {} }
DI int wave_max(int v) {
  unsigned u = (unsigned)v;
  u = max(u, (unsigned)__builtin_amdgcn_update_dpp(0, u, 0x111, 0xf, 0xf, false)); u = max(u, (unsigned)__builtin_amdgcn_update_dpp(0, u, 0x112, 0xf, 0xf, false));
  u = max(u, (unsigned)__builtin_amdgcn_update_dpp(0, u, 0x114, 0xf, 0xf, false)); u = max(u, (unsigned)__builtin_amdgcn_update_dpp(0, u, 0x118, 0xf, 0xf, false));
  return (int)max(max((unsigned)__builtin_amdgcn_readlane(u, 15), (unsigned)__builtin_amdgcn_readlane(u, 31)), max((unsigned)__builtin_amdgcn_readlane(u, 47), (unsigned)__builtin_amdgcn_readlane(u, 63)));
}
DI gu32* cw(const Ctx& c, int i) { return c.ctl + i * 32; }
DI bool aborted(const Ctx& c) { return rfl(__hip_atomic_load(cw(c, C_ABORT), RLX_AGENT)) != 0; }
DI void do_abort(const Ctx& c, unsigned code) { if (c.lane == 0) __hip_atomic_store(cw(c, C_ABORT), code, RLX_AGENT); }
DI bool spin_fail(const Ctx& c, unsigned& spins, unsigned code) {
  ++spins;
  if (spins > SPIN_CAP) { do_abort(c, code); return true; }
  return (spins & 255u) == 0u && aborted(c);
}
DI void wait_ge(const Ctx& c, int i, unsigned want, unsigned code, unsigned lazy = 0) {
  for (unsigned spins = 0;;) {
    const unsigned v = rfl(__hip_atomic_load(cw(c, i), RLX_AGENT));
    if (v >= want) break;
    if (lazy && v + lazy < want) __builtin_amdgcn_s_sleep(24); else __builtin_amdgcn_s_sleep(2);
    if (spin_fail(c, spins, code)) break;
  }
  asm volatile("" ::: "memory");
}
DI unsigned nearo(const Ctx& c, unsigned o) { return nmap(o) | ((((c.par >> (o >> 22)) ^ (o >> 20)) & 1u) << 13); }
DI unsigned faro(const Ctx& c, unsigned o) { return nearo(c, o) ^ 8192u; }
DI unsigned kxo(const Ctx& c, unsigned o) { return KXN ? nearo(c, o) : faro(c, o); }
DI unsigned xrec_soff(const Ctx& c, int s) { return nearo(c, N_XREC + (unsigned)s * 32768u); }
DI unsigned tp_soff(const Ctx& c, int s) { return nearo(c, N_TP + (unsigned)s * 1024u); }
DI unsigned um_soff(const Ctx& c, int s) { const int pg = s / 3; return nearo(c, N_UM + (unsigned)pg * MB + (unsigned)(s - pg * 3) * (320 * KB)); }
DI unsigned ux_soff(const Ctx& c, int s, int ct, int h) { return faro(c, F_UX + (unsigned)(s * 40 + ct) * 32768u) + nmap((unsigned)((h + UXROT * ct) & 31) * 1024u); }
DI unsigned km_soff(const Ctx& c, int s, int h) { return nearo(c, N_KM + (unsigned)(s * 32 + h) * 8192u); }

template <bool PI> DI void pfmt_voff(unsigned (&v)[3], int wave, int lane, unsigned pitch) {
#pragma unroll
  for (int i = 0; i < 3; ++i) {
    unsigned n = (wave * 3 + i) * 64 + lane, row = n / 24, pos = n % 24;
    unsigned key = PI ? ((row >> 1) & 3) : (((row >> 1) & 1) | (((row >> 3) & 1) << 1));
    v[i] = row * pitch + ((((pos >> 1) ^ key) << 1) | (pos & 1)) * 16;
  }
}
DI void ufmt_voff(unsigned (&v)[3], int wave, int lane) {
#pragma unroll
  for (int i = 0; i < 3; ++i) {
    unsigned n = (wave * 3 + i) * 64 + lane, row = n >> 3, pos = n & 7;
    v[i] = row * 1280u + ((((pos >> 1) ^ ((row >> 1) & 3)) << 1) | (pos & 1)) * 16;
  }
}
DI void stage_load(rsrc_t r, const unsigned (&v)[3], unsigned soff, unsigned slot, int wave) {
#pragma unroll
  for (int i = 0; i < 3; ++i) glds(r, v[i], soff, slot + (unsigned)(wave * 3 + i) * 1024u);
}
DI unsigned lane_kx(int lane) { return (((lane >> 4) & 1) << 1) | ((lane >> 3) & 1); }
template <bool PI> DI unsigned pfmt_base(int lane, int T) { int g = lane >> 4, q = (lane >> 2) & 3; return (unsigned)(((PI ? 4 : 8) * g + q) * 384 + ((T ^ lane_kx(lane)) * 32) + (lane & 3) * 8); }
DI unsigned ufmt_base(int lane, int C) { int g = lane >> 4, q = (lane >> 2) & 3; return (unsigned)((4 * g + q) * 128 + ((C ^ lane_kx(lane)) * 32) + (lane & 3) * 8); }
template <bool PI> DI bf16x8 pfmt_frag(unsigned a, int ks) { return cat4(trd(a + ks * 32 * 384), trd(a + ks * 32 * 384 + (PI ? 16 : 4) * 384)); }
DI bf16x8 ufmt_frag(unsigned a, int ks) { return cat4(trd(a + ks * 32 * 128), trd(a + ks * 32 * 128 + 16 * 128)); }
DI bf16x16 pfmt_frag64(unsigned a) { return cat8(pfmt_frag<false>(a, 0), pfmt_frag<false>(a, 1)); }
DI bf16x16 ufmt_frag64(unsigned a, int kk) { return cat8(ufmt_frag(a, 2 * kk), ufmt_frag(a, 2 * kk + 1)); }

constexpr unsigned L_MEM = 120 * KB, L_Q = 130 * KB, L_K = 136 * KB, L_KX = 142 * KB, L_MISC = 154 * KB;
DI constexpr unsigned slot_off(int i) { return i == 0 ? 0u : i == 1 ? 72 * KB : i == 2 ? 24 * KB : i == 3 ? 120 * KB : i == 4 ? 48 * KB : 96 * KB; }
#define OPQ(x) asm volatile("" : "+v"(x))
#define SB() __builtin_amdgcn_sched_barrier(0)
#define XPIN2(x) asm volatile("" : "+v"(x))

#define STAMP(c, role, s, k) do {} while (0)
#define STAMP_DECL do {} while (0)
#define FAILS(w) do {} while (0)
#define FAIL0(s) do {} while (0)
#define STAMP_FLUSH(cond, s, mask) do {} while (0)
#define STAMP_DUMP(c, role) do {} while (0)
#define TS_ROLE 0
#define TSD unsigned long long tt[10] = {}
#define TS(r, i) do { if (TS_ROLE == (r)) { SB(); asm volatile("s_memtime %0" : "=s"(tt[i])); SB(); } } while (0)
#define TSE(r) do { if (TS_ROLE == (r)) { asm volatile("s_waitcnt lgkmcnt(0)" : "+s"(tt[0]), "+s"(tt[1]), "+s"(tt[2]), "+s"(tt[3]), "+s"(tt[4]), "+s"(tt[5]), "+s"(tt[6]), "+s"(tt[7]), "+s"(tt[8]), "+s"(tt[9])); \
    if (s == 50 && h == 5) { for (int j = 0; j < 10; ++j) ((GAS unsigned*)((char*)c.ctl + (WS_DBG - WS_CTL) + MB))[w * 16 + j] = tt[j] ? (unsigned)(tt[j] - tt[0]) : 0u; } } } while (0)
constexpr unsigned L_STAMP = 157 * KB + 512;

#define POLL(c, bud, code, ...) do { bool ok; \
    _Pragma("nounroll") do { asm volatile("" ::: "memory"); __VA_ARGS__; } while (!ok && --(bud) > 0); \
    if (__builtin_expect(!ok, 0)) do_abort(c, code); } while (0)
DI void poll_mem(const Ctx& c, int& bud, int s, unsigned l16, int u, unsigned la) {
  const unsigned so = xrec_soff(c, s) + u * 1024;
  la += u * 1024;
  if (u < 2) {
    POLL(c, bud, 0x100 + s, { u32x4 a = ld16c(c.ws, l16, so); u32x4 b = ld16c(c.ws, l16, so + 4096); u32x4 d = ld16c(c.ws, l16, so + 16384);
      *(LAS u32x4*)(la) = a; *(LAS u32x4*)(la + 4096) = b; *(LAS u32x4*)(la + 8192) = d;
      ok = __all(umax4(umax4(umax4(0u, a), b), d) != SENT); });
  } else {
    POLL(c, bud, 0x100 + s, { u32x4 a = ld16c(c.ws, l16, so); u32x4 b = ld16c(c.ws, l16, so + 4096);
      *(LAS u32x4*)(la) = a; *(LAS u32x4*)(la + 4096) = b;
      ok = __all(umax4(umax4(0u, a), b) != SENT); });
  }
}
DI int last_pub(const Ctx& c, int s, unsigned off, unsigned n, unsigned l16) { OPQ(l16); return wave_max((int)ld4c(c.ws, min(l16 >> 2, (n - 1) * 4), tp_soff(c, s) + off)); }
DI int last_arr(const Ctx& c, int s, unsigned l16) { OPQ(l16); return wave_max((int)ld4c(c.ws, (l16 << 2) & 2047u, xrec_soff(c, s) + nmap(TA_OFF))); }
DI int wait_arr(const Ctx& c, int s, unsigned l16) {
  int t = last_arr(c, s, l16);
  for (int i = 0; i < 64 && t < 0; ++i) { asm volatile("" ::: "memory"); __builtin_amdgcn_s_sleep(2); t = last_arr(c, s, l16); }
  return t;
}
DI int pace_sv(const Ctx& c, int s, int& ta0, int& ze, unsigned l16, int vis) {
  if (s + 1 >= NS_) return 0;
  OPQ(l16);
  const unsigned vm = ld4c(c.ws, min(l16 >> 2, 156u), tp_soff(c, s) + TM_OFF), va = ld4c(c.ws, (l16 << 2) & 2047u, xrec_soff(c, s) + nmap(TA_OFF));
  const int tm = wave_max((int)vm);
  if (tm >= 0 && ta0 >= 0) ze = min(tm - ta0, ze + LEAK);
  ta0 = wave_max((int)va);
  if (ta0 < 0) ta0 = wait_arr(c, s, l16);
  return ta0 >= 0 && ze < 400 ? ta0 + ze + vis : 0;
}
DI bf16x16 ldsr32(unsigned a) { return cat8(ldsr16(a), ldsr16(a + 1024)); }
DI void ldsw32(unsigned a, bf16x16 v) {
  *(LAS bf16x8*)a = __builtin_shufflevector(v, v, 0, 1, 2, 3, 4, 5, 6, 7); *(LAS bf16x8*)(a + 1024) = __builtin_shufflevector(v, v, 8, 9, 10, 11, 12, 13, 14, 15);
}
DI f32x2 hsum(f32x4 v) { float a = v[0] + v[2], b = v[1] + v[3]; OPQ(a); OPQ(b); return f32x2{a, b}; }
DI f32x2 smul(float a, float b, float k) { a *= k; b *= k; OPQ(a); OPQ(b); return f32x2{a, b}; }
DI float x2(float v) { return __uint_as_float(__builtin_amdgcn_update_dpp(0u, __float_as_uint(v), 0x4E, 0xf, 0xf, true)); }

template <int KIND> DI void s_wave(const Params& p, const Ctx& c, int h) {
  constexpr bool QW = KIND != 2;
  constexpr int NR = QW ? 7 : 8;
  constexpr unsigned LB = QW ? 0 : 72 * KB;
  const int lane = c.lane, w = c.wave, u = w & 3;
  bf16x16 W[NR][3];
  unsigned wl = LB + u * 6144 + lane * 16; OPQ(wl);
  {
    const unsigned tl = pfmt_base<false>(lane, u);
    rsrc_t rq = mk_rsrc(p.Wq, D_ * H_ * E_ * 2), rk = mk_rsrc(p.Wk, D_ * H_ * E_ * 2);
    unsigned v[3]; pfmt_voff<false>(v, w, lane, 12288);
    const unsigned s0 = h * 384;
#pragma unroll
    for (int i = 0; i < 2; ++i) { stage_load(rq, v, s0 + i * 64 * 12288, slot_off(2 * i), w); stage_load(rk, v, s0 + i * 64 * 12288, slot_off(2 * i + 1), w); }
#pragma unroll
    for (int i = 0; i < 10; ++i) {
      if (i < 9) WAIT_V(6); else WAIT_V(0);
      LBAR();
      if (i + 2 < 10) { stage_load(rq, v, s0 + (i + 2) * 64 * 12288, slot_off(2 * ((i + 2) % 3)), w); stage_load(rk, v, s0 + (i + 2) * 64 * 12288, slot_off(2 * ((i + 2) % 3) + 1), w); }
      const unsigned so = slot_off(2 * (i % 3) + (QW ? 0 : 1));
      bf16x16 f[3];
#pragma unroll
      for (int t = 0; t < 3; ++t) f[t] = pfmt_frag64(tl + t * 128 + so);
      if (i >= 7) LBAR();
#pragma unroll
      for (int t = 0; t < 3; ++t) { if (i < NR) W[i < NR ? i : 0][t] = f[t]; else ldsw32(wl + so - LB + t * 2048, f[t]); }
    }
    LBAR();
  }
  const int idx = (lane & 2) ? 0xEEEE : 0x4444;
  int bud = 1 << 20; STAMP_DECL;
  const int g = lane >> 4, r16 = lane & 15;
  unsigned l16 = lane * 16; OPQ(l16); l16 &= 0x3F0u;
  unsigned memr = L_MEM + lane * 16; OPQ(memr);
  unsigned qkw = (QW ? L_Q : L_K) + (16 * u + r16) * 32 + ((g ^ (r16 >> 2)) * 8); OPQ(qkw);
  unsigned qkr = L_Q + (4 * g + (r16 >> 2)) * 32 + (((lane & 3) ^ g) * 8);
  unsigned kxa = L_KX + ((r16 >> 2) & 1) * 6144 + (g * 16 + 8 * u + 4 * (r16 >> 3) + (r16 & 3)) * 16;
  unsigned pxo = nmap(PCX_OFF + h * 512) + u * 256 + r16 * 16 + g * 4, pmo = nmap(PCM_OFF + h * 128) + ((r16 & 13) | ((g & 1) << 1)) * 8 + (g >> 1) * 4;
  unsigned exw = L_MISC + u * 1024 + lane * 16;
  if (KIND < 2) OPQ(qkr);
  if (KIND == 0) { OPQ(kxa); OPQ(pxo); OPQ(pmo); OPQ(exw); }
  volatile LAS unsigned* qflag = (volatile LAS unsigned*)(L_MISC + 2048);
  if (QW) qflag[u] = 0;
  const unsigned kxw = memr + (L_KX - L_MEM);
  if (KIND == 3) {
    wait_ge(c, C_FK + h, 1, 0x200);
#pragma unroll
    for (int i = 0; i < 12; ++i) *(LAS u32x4*)(kxw + i * 1024) = ld16c(c.ws, l16, kxo(c, F_KX + (h * 12 + i) * 1024));
  }
  const bool hi2 = lane & 2;
  int shot = 0, ref0 = -1, y0p = ZE0;
  const float sc = 0.07216878364870322f * 1.4426950408889634f;
  for (int s = 0; s < NS_; ++s) {
    bf16x16 bt[3];
#pragma unroll
    for (int t = 0; t < 3; ++t) bt[t] = ldsr32(wl + slot_off(2 * (NR % 3) + (QW ? 0 : 1)) - LB + t * 2048);
    f32x4 acc[3] = {};
    OPQ(acc[0]); OPQ(acc[1]); OPQ(acc[2]);
    unsigned so = xrec_soff(c, s); asm volatile("" : "+s"(so));
    unsigned kb[3];
    if (KIND == 3) {
      const int sn = s + 1 < NS_ ? s + 1 : s;
#pragma unroll
      for (int j = 0; j < 3; ++j) { kb[j] = kxo(c, F_KX + (sn * 32 + h) * 12288 + j * 4096); asm volatile("" : "+s"(kb[j])); }
    }
    SB();
    if (!QW) { wait_until(c, shot); STAMP(c, h, s, 3); poll_mem(c, bud, s, l16, u, memr); STAMP(c, h, s, 4); }
    if (KIND == 1) WAIT_V(0);
    TSD;
    LBAR();
    TS(1, 0);
    if (KIND == 2 && w == 7) st4c((unsigned)now(c), c.ws, 0, so + nmap(TA_OFF) + h * 64);
    {
      constexpr int ord[10] = {NR, 0, 1, 2, QW ? 8 : 3, QW ? 3 : 4, QW ? 4 : 5, QW ? 5 : 6, QW ? 6 : 7, 9};
      constexpr int AWIN = QW ? 4 : 3;
      bf16x8 a[10];
#pragma unroll
      for (int i = 0; i < AWIN; ++i) { a[i] = ldsr16(memr + ord[i] * 1024); SB(); }
      STAMP(c, h, s, 0);
#pragma unroll
      for (int i = 0; i < 10; ++i) {
        const int ks = ord[i];
#pragma unroll
        for (int t = 0; t < 3; ++t) {
          acc[t] = SMF(a[i], ks < NR ? W[ks < NR ? ks : 0][t] : bt[t], acc[t], idx);
          OPQ(acc[t]);
          SB();
          if (ks >= NR && ks < 9) { bt[t] = ldsr32(wl + slot_off(2 * ((ks + 1) % 3) + (QW ? 0 : 1)) - LB + t * 2048); SB(); }
        }
        if (i + AWIN < 10) { a[i + AWIN] = ldsr16(memr + ord[i + AWIN] * 1024); SB(); }
      }
    }
    TS(1, 1);
#pragma unroll
    for (int t = 0; t < 3; ++t) { const f32x2 r = hsum(acc[t]); const unsigned d = pk2(r[0], r[1]); *(LAS u32x2*)(qkw + t * 2048) = u32x2{d, d}; }
    if (QW) { asm volatile("" ::: "memory"); qflag[u] = (unsigned)(s + 1); }
    TS(1, 2);
    bf16x8 qf[6]; f32x2 y, ex; float lx;
    u32x4 kxn[12];
    if (KIND == 0) {
      bf16x8 kx[6];
      SB();
#pragma unroll
      for (int m = 0; m < 6; ++m) kx[m] = ldsr16(kxa + m * 1024);
      SB();
      for (int sp = 0; sp < (1 << 22); ++sp) { const u32x4 f = *(volatile LAS u32x4*)qflag; if (rfl(min(min(f[0], f[1]), min(f[2], f[3]))) > (unsigned)s) break; }
      asm volatile("" ::: "memory");
      TS(1, 3);
#pragma unroll
      for (int m = 0; m < 6; ++m) qf[m] = cat4(trd(qkr + m * 1024), trd(qkr + m * 1024 + 512));
      SB();
      f32x4 sx = {};
#pragma unroll
      for (int m = 0; m < 6; ++m) sx = MFMA(kx[m], qf[m], sx);
      OPQ(sx); TS(1, 4);
      y = smul(hi2 ? sx[2] : sx[0], hi2 ? sx[3] : sx[1], sc);
      ex = f32x2{__builtin_amdgcn_exp2f(y[0]), __builtin_amdgcn_exp2f(y[1])};
      lx = ex[0] + ex[1];
      f32x4 o = {lx, y[0], y[1], lx};
      *(LAS f32x4*)exw = o;
    }
    if (KIND == 1) { if (lane == 0 && s > 0) __hip_atomic_store(cw(c, C_PS + h), (unsigned)s, RLX_AGENT); }
    if (KIND == 3) {
      OPQ(l16); __builtin_assume(l16 < 1024u);
#pragma unroll
      for (int i = 0; i < 12; ++i) kxn[i] = ld16c(c.ws, l16 + (i & 3) * 1024, kb[i >> 2]);
    }
    TS(1, 5);
    LBAR();
    TS(1, 6);
    if (!QW) shot = pace_sv(c, s, ref0, y0p, l16, VIS_S);
    if (KIND == 0) {
      if (s + 1 < NS_) {
        STAMP(c, h, s, 1);
        bf16x8 kf[6];
#pragma unroll
        for (int m = 0; m < 6; ++m) { kf[m] = cat4(trd(qkr + (L_K - L_Q) + m * 1024), trd(qkr + (L_K - L_Q) + m * 1024 + 512)); SB(); }
        const float lo = *(LAS float*)(exw ^ 1024);
        SB();
        f32x4 sm = {};
#pragma unroll
        for (int m = 0; m < 6; ++m) sm = MFMA(kf[m], qf[m], sm);
        lx += lo; OPQ(lx); OPQ(sm); TS(1, 7);
        const f32x2 z = smul(sm[0], sm[1], sc);
        f32x2 em = {__builtin_amdgcn_exp2f(z[0]), __builtin_amdgcn_exp2f(z[1])};
        float l = lx + 0.5f * (em[0] + em[1]); l += x2(l); l = x32sum(x16sum(l));
        if (__builtin_expect(!__all(__float_as_uint(l) - 0x21800000u < 0x50000000u), 0)) {
          const f32x4 o = *(LAS f32x4*)(exw ^ 1024);
          float mx = fmaxf(fmaxf(fmaxf(y[0], y[1]), fmaxf(o[1], o[2])), fmaxf(z[0], z[1]));
          mx = fmaxf(mx, x2(mx)); mx = x32max(x16max(mx));
          ex[0] = __builtin_amdgcn_exp2f(y[0] - mx); ex[1] = __builtin_amdgcn_exp2f(y[1] - mx);
          em[0] = __builtin_amdgcn_exp2f(z[0] - mx); em[1] = __builtin_amdgcn_exp2f(z[1] - mx);
          l = ((ex[0] + ex[1]) + (__builtin_amdgcn_exp2f(o[1] - mx) + __builtin_amdgcn_exp2f(o[2] - mx))) + 0.5f * (em[0] + em[1]);
          l += x2(l); l = x32sum(x16sum(l));
          if (!(l == l)) { l = 1.0f; ex[0] = ex[1] = em[0] = em[1] = __uint_as_float(0x7FC00000u); }
        }
        const float inv = __builtin_amdgcn_rcpf(l);
        ex = smul(ex[0], ex[1], inv);
        st4c(pk2(ex[0], ex[1]), c.ws, pxo, so);
        if (u == 1) {
          em = smul(em[0], em[1], inv);
          st4c(pk2(em[0], em[1]), c.ws, pmo, so);
          st4c((unsigned)now(c), c.ws, 0, tp_soff(c, s) + TS_OFF + h * 4);
        }
        STAMP(c, h, s, 2);
        TS(1, 8);
      }
    }
    if (KIND == 1) {
      __builtin_amdgcn_s_sleep(4);
#pragma unroll
      for (int m = 0; m < 6; ++m) st16c(__builtin_bit_cast(u32x4, cat4(trd(qkr + (L_K - L_Q) + m * 1024), trd(qkr + (L_K - L_Q) + m * 1024 + 512))), c.ws, l16, km_soff(c, s, h) + m * 1024);
    }
    if (KIND == 3) {
      __builtin_amdgcn_s_sleep(4);
#pragma unroll
      for (int i = 0; i < 12; ++i) *(LAS u32x4*)(kxw + i * 1024) = kxn[i];
      if (((s + 2) & 7) == 0 && s + 2 < NS_) wait_ge(c, C_FK + ((s + 2) >> 3) * 32 + h, 1, 0x201);
    }
    TSE(1);
    FAIL0(s); STAMP_FLUSH(w == 0 || w == 4, s, (QW ? 7 : 24));
  }
  FAILS(w);
  if (KIND == 1) { WAIT_V(0); if (lane == 0) __hip_atomic_store(cw(c, C_PS + h), (unsigned)NS_, RLX_AGENT); }
}

template <bool AW> DI void v_wave(const Params& p, const Ctx& c, int h) {
  constexpr int N1 = AW ? 2 : 1, N2R = AW ? 2 : 5;
  const int lane = c.lane, w = c.wave, u = w & 3;
  bf16x16 W1[8][N1], W2[N2R][3];
  unsigned w1l = 72 * KB + (AW ? u * 4096 : 16384 + u * 2048) + lane * 16; OPQ(w1l);
  unsigned w2l = u * 6144 + lane * 16;
  if (AW) OPQ(w2l);
  {
    const unsigned tl = pfmt_base<false>(lane, AW ? u : u + 8);
    const int gb = lane >> 4, qq = (lane >> 2) & 3;
    const unsigned ur = (32 * (gb & 1) + 8 * (gb >> 1) + 2 * (qq & 1) + (qq >> 1)) * 128 + (lane & 3) * 8;
    const unsigned ul0 = ur + ((u ^ (qq & 1)) * 32), ul1 = ur + ((u ^ (2 + (qq & 1))) * 32) + 4 * 128;
    rsrc_t rv = mk_rsrc(p.Wv, D_ * H_ * E_ * 2), ro = mk_rsrc(p.Wo, D_ * H_ * E_ * 2);
    unsigned v[3], vu[3]; pfmt_voff<false>(v, w, lane, 12288); ufmt_voff(vu, w, lane);
    const unsigned s0 = h * 384, u0 = h * 192 * 1280;
#pragma unroll
    for (int i = 0; i < 2; ++i) { stage_load(ro, vu, u0 + i * 128, slot_off(2 * i), w); stage_load(rv, v, s0 + i * 64 * 12288, slot_off(2 * i + 1), w); }
#pragma unroll
    for (int i = 0; i < 10; ++i) {
      if (i < 9) WAIT_V(6); else WAIT_V(0);
      LBAR();
      if (i + 2 < 10) { stage_load(ro, vu, u0 + (i + 2) * 128, slot_off(2 * ((i + 2) % 3)), w); stage_load(rv, v, s0 + (i + 2) * 64 * 12288, slot_off(2 * ((i + 2) % 3) + 1), w); }
      bf16x16 f1[N1], f2[3];
      const bool mine = AW ? i >= 5 : i < 5;
#pragma unroll
      for (int t = 0; t < N1; ++t) f1[t] = pfmt_frag64(tl + t * 128 + slot_off(2 * (i % 3) + 1));
      if (mine) {
#pragma unroll
        for (int kk = 0; kk < 3; ++kk) { const unsigned o = slot_off(2 * (i % 3)) + kk * 64 * 128; f2[kk] = cat8(cat4(trd(ul0 + o), trd(ul1 + o)), cat4(trd(ul0 + o + 16 * 128), trd(ul1 + o + 16 * 128))); }
      }
      if (i >= 7) LBAR();
#pragma unroll
      for (int t = 0; t < N1; ++t) { if (i < 8) W1[i < 8 ? i : 0][t] = f1[t]; else ldsw32(w1l + slot_off(2 * (i % 3) + 1) - 72 * KB + t * 2048, f1[t]); }
      if (mine) {
#pragma unroll
        for (int kk = 0; kk < 3; ++kk) { if (i < 7) W2[AW ? (i == 5 ? 0 : 1) : (i < 5 ? i : 0)][kk] = f2[kk]; else ldsw32(w2l + slot_off(2 * (i % 3)) + kk * 2048, f2[kk]); }
      }
    }
    LBAR();
  }
  const int idx = (lane & 2) ? 0xEEEE : 0x4444;
  int bud = 1 << 20; STAMP_DECL;
  const int g = lane >> 4, r16 = lane & 15;
  unsigned l16 = lane * 16; OPQ(l16); l16 &= 0x3F0u;
  unsigned memr = L_MEM + lane * 16; OPQ(memr);
  unsigned vw = L_Q + ((AW ? u : u + 8) * 8 + (r16 >> 1)) * 32 + g * 8 + (r16 & 1) * 4; OPQ(vw);
  unsigned vr = L_Q + (4 * g + (r16 >> 2)) * 32 + (lane & 3) * 8; OPQ(vr);
  unsigned stv = h * 256 + r16 * 16 + g * 65536 + u * 16384 + (AW ? 5 * 65536 : 0); OPQ(stv);
  unsigned st1 = h * 256 + r16 * 16 + g * 4 + u * 16384 + (AW ? 9 * 65536 : 4 * 65536); OPQ(st1);
  int shot = 0, tm0 = -1, y0p = ZE0;
  for (int s = 0; s < NS_; ++s) {
    bf16x16 bt[N1];
#pragma unroll
    for (int t = 0; t < N1; ++t) bt[t] = ldsr32(w1l + slot_off(5) - 72 * KB + t * 2048);
    f32x4 acc[N1] = {};
#pragma unroll
    for (int t = 0; t < N1; ++t) OPQ(acc[t]);
    unsigned so = um_soff(c, s); asm volatile("" : "+s"(so));
    f32x4 oz[5] = {};
#pragma unroll
    for (int j = 0; j < 5; ++j) OPQ(oz[j]);
    SB();
    if (!AW) { wait_until(c, shot); poll_mem(c, bud, s, l16, u, memr); }
    WAIT_V(0);
    TSD;
    LBAR();
    TS(2, 0);
    {
      constexpr int ord[10] = {8, 0, 1, 2, 3, 4, 5, 6, 7, 9};
      constexpr int AWIN = AW ? 4 : 6;
      bf16x8 a[10];
#pragma unroll
      for (int i = 0; i < AWIN; ++i) { a[i] = ldsr16(memr + ord[i] * 1024); SB(); }
      STAMP(c, 32 + h, s, 0);
#pragma unroll
      for (int i = 0; i < 10; ++i) {
        const int ks = ord[i];
#pragma unroll
        for (int t = 0; t < N1; ++t) {
          acc[t] = SMF(a[i], ks < 8 ? W1[ks < 8 ? ks : 0][t] : bt[t], acc[t], idx);
          OPQ(acc[t]); SB();
          if (ks == 8) { bt[t] = ldsr32(w1l + slot_off(1) - 72 * KB + t * 2048); SB(); }
        }
        if (i + AWIN < 10) { a[i + AWIN] = ldsr16(memr + ord[i + AWIN] * 1024); SB(); }
      }
    }
    TS(2, 1);
#pragma unroll
    for (int t = 0; t < N1; ++t) { const f32x2 r = hsum(acc[t]); *(LAS unsigned*)(vw + t * 1024) = pk2(r[0], r[1]); }
    SB();
    TS(2, 2);
    bf16x16 ut[3];
    if (AW) {
#pragma unroll
      for (int kk = 0; kk < 3; ++kk) ut[kk] = ldsr32(w2l + slot_off(2) + kk * 2048);
    }
    if (AW && w == 0 && lane == 0 && s > 0) __hip_atomic_store(cw(c, C_PV + h), (unsigned)s, RLX_AGENT);
    TS(2, 3);
    LBAR();
    TS(2, 4);
    bf16x8 va[3];
#pragma unroll
    for (int kk = 0; kk < 3; ++kk) { va[kk] = cat4(trd(vr + kk * 1024), trd(vr + kk * 1024 + 512)); SB(); }
    SB();
    auto tile = [&](const bf16x16 (&b)[3], int j) { f32x4 o = oz[j];
#pragma unroll
      for (int kk = 0; kk < 3; ++kk) o = SMF(va[kk], b[kk], o, idx);
      OPQ(o); SB();
      return o; };
    auto refill = [&](unsigned off) {
#pragma unroll
      for (int kk = 0; kk < 3; ++kk) ut[kk] = ldsr32(w2l + off + kk * 2048);
      SB(); };
    auto pk = [&](f32x4 o) { const f32x2 r = hsum(o); unsigned d = pk2s(r[0], r[1]); OPQ(d); SB(); return d; };
    auto put4 = [&](unsigned d0, unsigned d1, unsigned d2, unsigned d3) {
      const u32x2 x = __builtin_amdgcn_permlane16_swap(d0, d1, false, false), y = __builtin_amdgcn_permlane16_swap(d2, d3, false, false);
      const u32x2 e = __builtin_amdgcn_permlane32_swap(x[0], y[0], false, false), f = __builtin_amdgcn_permlane32_swap(x[1], y[1], false, false);
      st16c(u32x4{e[0], f[0], e[1], f[1]}, c.ws, stv, so); SB(); };
    if (AW) {
      f32x4 o7 = tile(ut, 0); refill(slot_off(4));
      f32x4 o5 = tile(W2[0], 1); const unsigned d7 = pk(o7);
      f32x4 o6 = tile(W2[1], 2); const unsigned d5 = pk(o5);
      f32x4 o8 = tile(ut, 3); refill(slot_off(0)); const unsigned d6 = pk(o6), d8 = pk(o8);
      f32x4 o9 = tile(ut, 4);
      TS(2, 5);
      put4(d5, d6, d7, d8);
      st4c(pk(o9), c.ws, st1, so);
    } else {
      f32x4 o4 = tile(W2[AW ? 0 : 4], 4);
      f32x4 o0 = tile(W2[0], 0); st4c(pk(o4), c.ws, st1, so);
      f32x4 o1 = tile(W2[1], 1); const unsigned d0 = pk(o0);
      f32x4 o2 = tile(W2[AW ? 0 : 2], 2); const unsigned d1 = pk(o1);
      f32x4 o3 = tile(W2[AW ? 0 : 3], 3); const unsigned d2 = pk(o2);
      TS(2, 5);
      put4(d0, d1, d2, pk(o3));
    }
    STAMP(c, 32 + h, s, 2);
    TS(2, 7); TSE(2);
    if (!AW) shot = pace_sv(c, s, tm0, y0p, l16, VIS_V);
    if (AW && s + 2 < NS_) {
      __builtin_amdgcn_s_sleep(RST_DLY);
      const unsigned sr = um_soff(c, s + 2) + h * 256 + u * 16384; const u32x4 d = {SENT, SENT, SENT, SENT};
      unsigned rv = l16; OPQ(rv); rv = (rv & 255u) + (rv >> 8) * 65536;
      st16c(d, c.ws, rv, sr); st16c(d, c.ws, rv, sr + 4 * 65536); if (lane < 32) st16c(d, c.ws, rv, sr + 8 * 65536);
    }
    FAIL0(s); STAMP_FLUSH(w == 4, s, 5);
  }
  FAILS(w);
  WAIT_V(0);
  BAR();
  if (w == 0 && lane == 0) __hip_atomic_store(cw(c, C_PV + h), (unsigned)NS_, RLX_AGENT);
}

DI void m_wave(const Params& p, const Ctx& c, int ct) {
  const int lane = c.lane, w = c.wave;
  const int idx = (lane & 2) ? 0xEEEE : 0x4444;
  int bud = 1 << 20; STAMP_DECL;
  const int g = lane >> 4, r16 = lane & 15;
  unsigned l16 = lane * 16; OPQ(l16); l16 &= 0x3F0u;
  unsigned vm = g * 256 + r16 * 8; OPQ(vm); vm &= 0x378u;
  unsigned vpub = nmap(ct * 256) + (4 * g + (r16 & 3)) * 16 + (r16 >> 2) * 4; OPQ(vpub);
  unsigned zr = 64; OPQ(zr);
  volatile LAS unsigned* done = (volatile LAS unsigned*)(zr);
  if (w == 0) { done[0] = 0; done[4] = 0; done[1] = ~0u; done[2] = AE0; done[3] = PE0; }
  LBAR();
  for (int s = w; s < NS_ - 1; s += 8) {
    u32x4 bx[32], bm[8];
    const int sb = s - PFD;
    if (sb > 0) for (int sp = 0; rfl(done[0]) < (unsigned)sb && sp < (1 << 24); ++sp) __builtin_amdgcn_s_sleep(2);
    wait_ge(c, C_CU + (s >> 3), 320, 0x300, 40);
    if (sb >= 0) for (int sp = 0; rfl(done[4]) <= (unsigned)sb && sp < (1 << 24); ++sp) __builtin_amdgcn_s_sleep(1);
    else for (int i = 0; i < s; ++i) __builtin_amdgcn_s_sleep(8);
#pragma unroll
    for (int i = 0; i < PF1; ++i) bx[i] = ld16c(c.ws, l16, ux_soff(c, s, ct, i & ~3) + (i & 3) * 1024);
    SB();
    if (sb >= 0) for (int sp = 0; rfl(done[0]) <= (unsigned)sb && sp < (1 << 24); ++sp) __builtin_amdgcn_s_sleep(1);
#pragma unroll
    for (int i = PF1; i < 32; ++i) bx[i] = ld16c(c.ws, l16, ux_soff(c, s, ct, i & ~3) + (i & 3) * 1024);
    SB();
    for (int sp = 0; rfl(done[0]) < (unsigned)s && sp < (1 << 24); ++sp) __builtin_amdgcn_s_sleep(2);
    unsigned so = xrec_soff(c, s), su = um_soff(c, s) + ct * 16384, sn = xrec_soff(c, s + 1), sm = so + nmap(PCM_OFF);
    unsigned sx[4] = {so + nmap(PCX_OFF), so + nmap(PCX_OFF + 4096), so + nmap(PCX_OFF + 8192), so + nmap(PCX_OFF + 12288)}, su2 = su + 4096;
    asm volatile("" : "+s"(sm), "+s"(su), "+s"(sn), "+s"(su2), "+s"(sx[0]), "+s"(sx[1]), "+s"(sx[2]), "+s"(sx[3]));
    {
      int ta1 = (int)rfl(done[1]), ae = (int)rfl(done[2]), pe = (int)rfl(done[3]);
      const int ts1 = s > 0 ? last_pub(c, s - 1, TS_OFF, 32, l16) : -1;
      if (ts1 >= 0 && ta1 >= 0) ae = min(ts1 - ta1, ae + LEAK);
      if (ta1 >= 0 && pe < 400) wait_until(c, ta1 + pe + PRE_M);
      const int ta = wait_arr(c, s, l16);
      if (ta >= 0 && ta1 >= 0) pe = min(ta - ta1, pe + LEAK);
      done[1] = (unsigned)ta; done[2] = (unsigned)ae; done[3] = (unsigned)pe;
      if (ta >= 0 && ae < 300) wait_until(c, ta + ae + VIS_M);
    }
    STAMP(c, 64 + ct, s, 0);
    unsigned res;
    POLL(c, bud, 0x500 + s, {
      u32x4 ax[16]; u32x2 am[8];
      OPQ(l16); OPQ(vm); __builtin_assume(l16 < 1024u); __builtin_assume(vm < 1024u);
      if (!UM_LAST) { _Pragma("unroll") for (int i = 0; i < 8; ++i) bm[i] = ld16c(c.ws, l16 + (i & 3) * 1024, i < 4 ? su : su2); }
      SB();
      _Pragma("unroll") for (int i = 0; i < 16; ++i) { ax[i] = ld16c(c.ws, l16 + (i & 3) * 1024, sx[i >> 2]); SB(); }
      _Pragma("unroll") for (int i = 0; i < 8; ++i) { am[i] = ld8c(c.ws, vm + (i >> 1) * 1024 + (i & 1) * 128, sm); SB(); }
      if (UM_LAST) { _Pragma("unroll") for (int i = 0; i < 8; ++i) { bm[i] = ld16c(c.ws, l16 + (i & 3) * 1024, i < 4 ? su : su2); SB(); } }
      done[4] = (unsigned)(s + 1);
      f32x2 zl, zh; asm volatile("v_mov_b64 %0, 0\n\tv_mov_b64 %1, 0\n\ts_nop 1" : "=v"(zl), "=v"(zh));
      f32x4 acc = __builtin_shufflevector(zl, zh, 0, 1, 2, 3);
      SB();
      _Pragma("unroll") for (int i = 0; i < 16; ++i) acc = SMF(__builtin_bit_cast(bf16x8, ax[i]), cat8(__builtin_bit_cast(bf16x8, bx[2 * i]), __builtin_bit_cast(bf16x8, bx[2 * i + 1])), acc, idx);
      _Pragma("unroll") for (int i = 0; i < 4; ++i) { u32x4 a4 = {am[2 * i][0], am[2 * i][1], am[2 * i + 1][0], am[2 * i + 1][1]};
        acc = SMF(__builtin_bit_cast(bf16x8, a4), cat8(__builtin_bit_cast(bf16x8, bm[2 * i]), __builtin_bit_cast(bf16x8, bm[2 * i + 1])), acc, idx); }
      const f32x2 hs = hsum(acc); const float s0 = hs[0]; const float s1 = hs[1];
      const bool odd = lane & 1;
      const float t = __uint_as_float(dpp_x1(__float_as_uint(odd ? s0 : s1)));
      res = pk2s(odd ? t : s0, odd ? s1 : t); OPQ(res);
      ok = __all(s0 == s0 && s1 == s1);
      if (!ok) { unsigned m = 0;
        _Pragma("unroll") for (int i = 0; i < 16; ++i) m = umax4(m, ax[i]);
        _Pragma("unroll") for (int i = 0; i < 8; ++i) m = umax4(umax3(m, am[i][0], am[i][1]), bm[i]);
        ok = __all(m != SENT); } });
    st4c(res, c.ws, vpub, sn);
    STAMP(c, 64 + ct, s, 2);
    st4c((unsigned)now(c), c.ws, 0, tp_soff(c, s + 1) + TM_OFF + ct * 4);
    done[0] = (unsigned)(s + 1);
    FAIL0(s); STAMP_FLUSH(true, s, 5);
  }
  FAILS(w);
}

template <bool SC> DI void xload(u32x4& d, rsrc_t r, unsigned v, unsigned s) {
  if (SC) asm volatile("buffer_load_dwordx4 %0, %1, %2, %3 offen sc1" : "=v"(d) : "v"(v), "s"(r), "s"(s) : "memory");
  else asm volatile("buffer_load_dwordx4 %0, %1, %2, %3 offen" : "=v"(d) : "v"(v), "s"(r), "s"(s) : "memory");
}
#define XPIN(a, b) asm volatile("" : "+v"(a), "+v"(b))
constexpr int WIN = 4;
constexpr unsigned STG = 24 * KB;

struct WStream { rsrc_t r; unsigned v[3]; unsigned s0, step; };

struct Ring { unsigned rb[4], cur[4], slot; };
DI void ring_next(Ring& r) {
  r.slot = r.slot == 2 * STG ? 0u : r.slot + STG;
#pragma unroll
  for (int j = 0; j < 4; ++j) { r.cur[j] = r.rb[j] + r.slot; OPQ(r.cur[j]); }
}
template <bool PI, int NST, bool XSC, int MODE>
DI void proj_stage(const Ctx& c, f32x4 (&acc)[12][2], u32x4 (&X)[2][2], bf16x8 (&A)[WIN], Ring& rg, int t, const WStream& ws, unsigned xs, unsigned xts, const WStream& nx, bool has_next, unsigned l16) {
  auto xl = [&](int ks) { xload<XSC>(X[ks][0], c.ws, l16, faro(c, xs + (2 * t + 4 + ks) * 1024)); xload<XSC>(X[ks][1], c.ws, l16, faro(c, xs + (xts + 2 * t + 4 + ks) * 1024)); };
#pragma unroll
  for (int q = 0; q < 24; ++q) {
    const int ks = q / 12, T = q % 12;
    if (q == 0) XPIN(X[0][0], X[0][1]);
    if (q == 12) {
      if (MODE == 1) { if (has_next) WAIT_V(7); else WAIT_V(4); } else if (MODE == 2) { if (has_next) WAIT_V(3); else WAIT_V(0); } else WAIT_V(7);
      XPIN(X[1][0], X[1][1]);
      if (MODE == 0) xl(0);
    }
    const bf16x8 a = A[q % WIN];
    acc[T][0] = MFMA(a, __builtin_bit_cast(bf16x8, X[ks][0]), acc[T][0]);
    acc[T][1] = MFMA(a, __builtin_bit_cast(bf16x8, X[ks][1]), acc[T][1]);
    XPIN(acc[T][0], acc[T][1]);
    SB();
    if (q == 23 && MODE == 0) xl(1);
    if (q + WIN == 24 && MODE != 2) {
      if (MODE == 1) { if (has_next) WAIT_V(5); else WAIT_V(2); } else WAIT_V(7);
      LBAR();
      if (MODE == 0 && t + 3 < NST) stage_load(ws.r, ws.v, ws.s0 + (t + 3) * ws.step, rg.slot, c.wave);
      else if (has_next) stage_load(nx.r, nx.v, nx.s0 + (MODE == 1 ? nx.step : 0u), rg.slot, c.wave);
      ring_next(rg);
      SB();
    }
    if (q + WIN < 24 || MODE != 2) {
      const int f = (q + WIN) % 24;
      A[q % WIN] = pfmt_frag<PI>(rg.cur[(f % 12) & 3] + ((f % 12) >> 2) * 128, f / 12);
      SB();
    }
  }
}
template <bool PI, int NST, bool XSC> DI void proj(const Ctx& c, f32x4 (&acc)[12][2], const WStream& ws, unsigned xs, unsigned xts, const WStream& nx, bool has_next, unsigned l16) {
  const int w = c.wave;
  Ring rg;
#pragma unroll
  for (int j = 0; j < 4; ++j) rg.rb[j] = pfmt_base<PI>(c.lane, j);
  u32x4 X0[2][2], X1[2][2];
  stage_load(ws.r, ws.v, ws.s0, 0, w);
#pragma unroll
  for (int i = 0; i < 4; ++i) xload<XSC>(X0[i >> 1][i & 1], c.ws, l16, faro(c, xs + ((i & 1) * xts + (i >> 1)) * 1024));
  stage_load(ws.r, ws.v, ws.s0 + ws.step, STG, w);
#pragma unroll
  for (int i = 0; i < 4; ++i) xload<XSC>(X1[i >> 1][i & 1], c.ws, l16, faro(c, xs + ((i & 1) * xts + 2 + (i >> 1)) * 1024));
  stage_load(ws.r, ws.v, ws.s0 + 2 * ws.step, 2 * STG, w);
#pragma unroll
  for (int T = 0; T < 12; ++T) { acc[T][0] = f32x4{}; acc[T][1] = f32x4{}; }
  bf16x8 A[WIN];
  rg.slot = 2 * STG; ring_next(rg);
  WAIT_V(12);
  LBAR();
#pragma unroll
  for (int f = 0; f < WIN; ++f) A[f] = pfmt_frag<PI>(rg.cur[f & 3] + (f >> 2) * 128, 0);
  SB();
#pragma nounroll
  for (int t = 0; t < NST - 2; t += 2) {
    proj_stage<PI, NST, XSC, 0>(c, acc, X0, A, rg, t, ws, xs, xts, nx, has_next, l16);
    proj_stage<PI, NST, XSC, 0>(c, acc, X1, A, rg, t + 1, ws, xs, xts, nx, has_next, l16);
  }
  proj_stage<PI, NST, XSC, 1>(c, acc, X0, A, rg, NST - 2, ws, xs, xts, nx, has_next, l16);
  proj_stage<PI, NST, XSC, 2>(c, acc, X1, A, rg, NST - 1, ws, xs, xts, nx, has_next, l16);
}

template <bool LAST> DI void u_stage(const Ctx& c, const bf16x8 (&VF)[6][2], f32x4 (&o)[2][2], bf16x8 (&B)[WIN], Ring& rg, int t, const WStream& ws, int s, int c0, int h, unsigned l16, int nu) {
#pragma unroll
  for (int q = 0; q < 24; ++q) {
    const int m = q % 6, C = q / 6;
    const bf16x8 b = B[q % WIN];
    o[C & 1][0] = MFMA(VF[m][0], b, m == 0 ? f32x4{} : o[C & 1][0]);
    o[C & 1][1] = MFMA(VF[m][1], b, m == 0 ? f32x4{} : o[C & 1][1]);
    XPIN(o[C & 1][0], o[C & 1][1]);
    SB();
    if (m == 1 && (C > 0 || t > 0)) { st16c(__builtin_bit_cast(u32x4, pack8(o[(C + 1) & 1][0], o[(C + 1) & 1][1])), c.ws, l16, ux_soff(c, s, c0 + 4 * t + C - 1, h)); SB(); }
    if (q + WIN == 24 && !LAST) {
      WAIT_V(6);
      LBAR();
      if (t + 3 < nu) stage_load(ws.r, ws.v, ws.s0 + (t + 3) * ws.step, rg.slot, c.wave);
      ring_next(rg);
      SB();
    }
    if (q + WIN < 24 || !LAST) { const int f = (q + WIN) % 24; B[q % WIN] = ufmt_frag(rg.cur[f / 6], f % 6); SB(); }
  }
}
DI void uphase(const Ctx& c, const f32x4 (&acc)[12][2], const WStream& ws, int s, int c0, int h, unsigned l16, int nu) {
  Ring rg;
#pragma unroll
  for (int j = 0; j < 4; ++j) rg.rb[j] = ufmt_base(c.lane, j);
  bf16x8 B[WIN];
  rg.slot = 0; ring_next(rg);
  WAIT_V(3);
  LBAR();
  stage_load(ws.r, ws.v, ws.s0 + 2 * ws.step, 0, c.wave);
#pragma unroll
  for (int f = 0; f < WIN; ++f) B[f] = ufmt_frag(rg.cur[f / 6], f % 6);
  SB();
  bf16x8 VF[6][2];
#pragma unroll
  for (int m = 0; m < 6; ++m) { VF[m][0] = pack8(acc[2 * m][0], acc[2 * m + 1][0]); VF[m][1] = pack8(acc[2 * m][1], acc[2 * m + 1][1]); XPIN(VF[m][0], VF[m][1]); }
  SB();
  f32x4 o[2][2] = {};
#pragma nounroll
  for (int t = 0; t < nu - 1; ++t) u_stage<false>(c, VF, o, B, rg, t, ws, s, c0, h, l16, nu);
  u_stage<true>(c, VF, o, B, rg, nu - 1, ws, s, c0, h, l16, nu);
  st16c(__builtin_bit_cast(u32x4, pack8(o[1][0], o[1][1])), c.ws, l16, ux_soff(c, s, c0 + 4 * nu - 1, h));
}

DI void signal_wg(const Ctx& c, int word, unsigned inc = 1) {
  WAIT_V(0);
  BAR();
  if (c.wave == 0 && c.lane == 0) __hip_atomic_fetch_add(cw(c, word), inc, RLX_AGENT);
}

DI void attn_tail(const Ctx& c, const f32x4 (&acc)[12][2], int s, int h, unsigned l16) {
  const int lane = c.lane, g = lane >> 4, tok = lane & 15;
  bf16x8 QF[6][2];
#pragma unroll
  for (int m = 0; m < 6; ++m) { QF[m][0] = pack8(acc[2 * m][0], acc[2 * m + 1][0]); QF[m][1] = pack8(acc[2 * m][1], acc[2 * m + 1][1]); }
  wait_ge(c, C_FK + (s >> 3) * 32 + h, 1, 0x600);
  f32x4 x00 = {}, x10 = {}, x11 = {}, m0 = {}, m1 = {};
  {
    u32x4 kx[12];
#pragma unroll
    for (int i = 0; i < 12; ++i) kx[i] = ld16c(c.ws, l16, kxo(c, F_KX + ((s * 32 + h) * 12 + i) * 1024));
    SB();
#pragma unroll
    for (int m = 0; m < 6; ++m) {
      x00 = MFMA(__builtin_bit_cast(bf16x8, kx[m]), QF[m][0], x00);
      x10 = MFMA(__builtin_bit_cast(bf16x8, kx[m]), QF[m][1], x10);
      x11 = MFMA(__builtin_bit_cast(bf16x8, kx[6 + m]), QF[m][1], x11);
    }
  }
  wait_ge(c, C_PS + h, s + 1, 0x601, 1);
  {
    u32x4 km[6];
#pragma unroll
    for (int i = 0; i < 6; ++i) km[i] = ld16c(c.ws, l16, km_soff(c, s, h) + i * 1024);
    SB();
#pragma unroll
    for (int m = 0; m < 6; ++m) { m0 = MFMA(__builtin_bit_cast(bf16x8, km[m]), QF[m][0], m0); m1 = MFMA(__builtin_bit_cast(bf16x8, km[m]), QF[m][1], m1); }
  }
  const float sc = 0.07216878364870322f * 1.4426950408889634f, NEG = -1.0e9f * 1.4426950408889634f;
#pragma unroll
  for (int a = 0; a < 2; ++a) {
    f32x4 lo = a ? x10 : x00, hi = x11, mm = a ? m1 : m0;
#pragma unroll
    for (int i = 0; i < 4; ++i) {
      mm[i] *= sc;
      const bool keep = 4 * g + i <= tok;
      if (a == 0) { lo[i] = keep ? lo[i] * sc : NEG; hi[i] = NEG; } else { lo[i] *= sc; hi[i] = keep ? hi[i] * sc : NEG; }
    }
    float mx = fmaxf(fmaxf(fmaxf(lo[0], lo[1]), fmaxf(lo[2], lo[3])), fmaxf(mm[0], mm[1]));
    mx = fmaxf(mx, fmaxf(fmaxf(hi[0], hi[1]), fmaxf(hi[2], hi[3])));
    mx = x32max(x16max(mx));
    mm[0] = __builtin_amdgcn_exp2f(mm[0] - mx); mm[1] = __builtin_amdgcn_exp2f(mm[1] - mx);
    float l = mm[0] + mm[1];
#pragma unroll
    for (int i = 0; i < 4; ++i) { lo[i] = __builtin_amdgcn_exp2f(lo[i] - mx); hi[i] = __builtin_amdgcn_exp2f(hi[i] - mx); l += lo[i] + hi[i]; }
    l = x32sum(x16sum(l));
    const float inv = 1.0f / l;
    const unsigned po = F_PX + (s * 2 + a) * 40960;
    st16c(__builtin_bit_cast(u32x4, pack8(lo * inv, hi * inv)), c.ws, l16, faro(c, po + h * 1024));
    st4c(pk2(mm[0] * inv, mm[1] * inv), c.ws, tok * 16 + g * 4, faro(c, po + 32768 + h * 256));
  }
  WAIT_V(0);
  if (lane == 0) __hip_atomic_fetch_add(cw(c, C_CP + s), 1u, RLX_AGENT);
}

DI void fin_item(const Params& p, const Ctx& c, int s, int part, unsigned l16) {
  const int lane = c.lane, w = c.wave, g = lane >> 4, tok = lane & 15;
  auto wait_words = [&](bool all) {
    const int word = lane < 32 ? C_PV + lane : lane == 32 ? C_CP + s : lane == 33 ? C_CU + (s >> 3) : C_CG + (s >> 3);
    const unsigned want = lane > 33 ? 4u : lane == 33 ? 320u : lane == 32 ? (all ? 32u : 0u) : all ? (unsigned)s + 1u : (unsigned)max(s - FIN_LEAD, 0);
    for (unsigned spins = 0;;) {
      const unsigned v = __hip_atomic_load(cw(c, word), RLX_AGENT);
      if (__all(v >= want)) break;
      if (__all(lane >= 32 || v + 1 >= want)) __builtin_amdgcn_s_sleep(2); else __builtin_amdgcn_s_sleep(32);
      if (spin_fail(c, spins, 0x700)) break;
    }
  };
  if (w == 0) wait_words(false);
  LBAR();
  const int ct = part * 8 + w;
  u32x4 u[40];
#pragma unroll
  for (int i = 0; i < 32; ++i) { u[i] = ld16c(c.ws, l16, ux_soff(c, s, ct, i & ~3) + (i & 3) * 1024); if (FIN_THR && i >= FIN_THR) OPQ(u[i - FIN_THR]); }
  f32x4 gt[2]; u32x2 xv[2];
#pragma unroll
  for (int a = 0; a < 2; ++a) {
    gt[a] = __builtin_bit_cast(f32x4, ld16c(c.ws, l16, faro(c, F_G + ((s * 2 + a) * 40 + ct) * 1024)));
    xv[a] = *(const u32x2*)(p.x + (size_t)(s * 32 + a * 16 + tok) * D_ + ct * 16 + g * 4);
  }
  SB();
  if (w == 0) wait_words(true);
  BAR();
  {
    u32x4 pv[10];
    const unsigned su = um_soff(c, s) + ct * 16384;
#pragma unroll
    for (int i = 0; i < 10; ++i) pv[i] = ld16c(c.ws, l16, faro(c, F_PX + s * 81920 + (w * 10 + i) * 1024));
#pragma unroll
    for (int i = 0; i < 8; ++i) u[32 + i] = ld16c(c.ws, l16, su + i * 1024);
    SB();
#pragma unroll
    for (int i = 0; i < 10; ++i) *(LAS u32x4*)(l16 + (w * 10 + i) * 1024) = pv[i];
  }
  LBAR();
  unsigned pa = l16, pb = l16 + 40960; OPQ(pa); OPQ(pb);
  f32x4 o0 = {}, o1 = {};
  bf16x8 b0[4], b1[4];
#pragma unroll
  for (int i = 0; i < 3; ++i) { b0[i] = ldsr16(pa + i * 1024); b1[i] = ldsr16(pb + i * 1024); }
#pragma unroll
  for (int i = 0; i < 40; ++i) {
    if (i + 3 < 40) { b0[(i + 3) & 3] = ldsr16(pa + (i + 3) * 1024); b1[(i + 3) & 3] = ldsr16(pb + (i + 3) * 1024); }
    SB();
    o0 = MFMA(__builtin_bit_cast(bf16x8, u[i]), b0[i & 3], o0); o1 = MFMA(__builtin_bit_cast(bf16x8, u[i]), b1[i & 3], o1);
    XPIN(o0, o1); SB();
  }
#pragma unroll
  for (int a = 0; a < 2; ++a) {
    const f32x4 o = a ? o1 : o0;
    u32x2 r = {pk2(b2f(xv[a][0] & 0xffffu) + o[0] * gt[a][0], __uint_as_float(xv[a][0] & 0xffff0000u) + o[1] * gt[a][1]),
               pk2(b2f(xv[a][1] & 0xffffu) + o[2] * gt[a][2], __uint_as_float(xv[a][1] & 0xffff0000u) + o[3] * gt[a][3])};
    *(u32x2*)(p.out + (size_t)(s * 32 + a * 16 + tok) * D_ + ct * 16 + g * 4) = r;
  }
}

enum { I_VU = 0, I_K = 1, I_F1 = 2, I_Q = 3, I_F2 = 4, I_FIN = 5 };
struct ItemTab { unsigned v[1280]; int n; };
constexpr unsigned item(int type, int rb, int h, int u0 = 0, int nu = 10) { return (unsigned)(type | rb << 3 | h << 6 | u0 << 12 | nu << 16); }
constexpr ItemTab make_items() {
  ItemTab t = {};
  int n = 0;
  auto grp = [&](int type, int rb, int cnt) { for (int h = 0; h < cnt; ++h) t.v[n++] = item(type, rb, h); };
  auto A = [&](int rb) { grp(I_VU, rb, 32); grp(I_K, rb, 32); };
  auto mix = [&](int q, int f0, int nf) {
    int fi = 0;
    for (int h = 0; h < 32; ++h) { t.v[n++] = item(I_Q, q, h); for (; fi < (h + 1) * nf * 40 / 32; ++fi) t.v[n++] = item(I_FIN, f0 + fi / 40, fi % 40); }
  };
  auto mixA = [&](int rb, int f) {
    int fi = 0;
    for (int i = 0; i < 64; ++i) { t.v[n++] = item(i < 32 ? I_VU : I_K, rb, i & 31); for (; fi < (i + 1) * 40 / 64; ++fi) t.v[n++] = item(I_FIN, f, fi); }
  };
  for (int h = 0; h < 32; ++h) { t.v[n++] = item(I_VU, 0, h, 0, 5); t.v[n++] = item(I_VU, 0, h, 5, 5); }
  grp(I_K, 0, 32); grp(I_VU, 1, 32); grp(I_F1, 0, 8); grp(I_F1, 1, 8); grp(I_K, 1, 32);
  grp(I_F2, 0, 4); grp(I_F2, 1, 4); A(2); grp(I_F1, 2, 8); grp(I_F1, 3, 8);
  grp(I_Q, 0, 32); grp(I_F2, 2, 4); grp(I_F2, 3, 4); A(3);
  for (int rb = 4; rb < 8; ++rb) grp(I_F1, rb, 8);
  A(4);
  for (int rb = 4; rb < 8; ++rb) grp(I_F2, rb, 4);
  grp(I_Q, 1, 32); mixA(5, 0); grp(I_Q, 2, 32); mixA(6, 1); grp(I_Q, 3, 32); mixA(7, 2);
  mix(4, 3, 1);
  for (int rb = 5; rb < 8; ++rb) { grp(I_Q, rb, 32); grp(I_FIN, rb - 1, 40); }
  grp(I_FIN, 7, 40);
  t.n = n;
  return t;
}
__device__ constexpr ItemTab ITEMS = make_items();
constexpr int N_ITEMS = make_items().n;
static_assert(N_ITEMS == 32 + 256 * 3 + 64 + 32 + 320, "item count");
DI int next_item(const Ctx& c) {
  LAS unsigned* tk = (LAS unsigned*)(LDS_BYTES - 16);
  LBAR();
  if (c.wave == 0 && c.lane == 0) *tk = __hip_atomic_fetch_add(cw(c, C_Q), 1u, RLX_AGENT);
  LBAR();
  return (int)rfl(*tk);
}
DI void bulk(const Params& p, const Ctx& c, int lo, int hi) {
  const int lane = c.lane, w = c.wave, g = lane >> 4;
  unsigned l16 = lane * 16; OPQ(l16); l16 &= 0x3F0u;
#define BSTAMP(k, v) do {} while (0)
  for (;;) {
    const int it = lo + next_item(c);
    if (it >= hi) break;
    const unsigned code = ITEMS.v[it];
    const int type = code & 7, rb = (code >> 3) & 7, h = (code >> 6) & 63, u0 = (code >> 12) & 15, nu = code >> 16;
    const int s = rb * 8 + w;
    if (type == 5) { fin_item(p, c, rb * 8 + h / 5, h % 5, l16); continue; }
    f32x4 acc[12][2];
    if (type == 4) {
      if (w == 0) wait_ge(c, C_F1 + rb, 8, 0x800);
      LBAR();
      const int n0 = h < 3 ? h * 192 : 448;
      WStream ws; ws.r = mk_rsrc(p.w2, F_ * D_ * 2); pfmt_voff<true>(ws.v, w, lane, 1280); ws.s0 = n0 * 2; ws.step = 64 * 1280;
      proj<true, 24, true>(c, acc, ws, F_HF + s * 2 * 48 * 1024, 48, ws, false, l16);
#pragma unroll
      for (int T = 0; T < 12; ++T) {
        const u32x2 bv = *(const u32x2*)(p.b2 + n0 + T * 16 + g * 4);
        const f32x4 bb = {b2f(bv[0] & 0xffffu), __uint_as_float(bv[0] & 0xffff0000u), b2f(bv[1] & 0xffffu), __uint_as_float(bv[1] & 0xffff0000u)};
#pragma unroll
        for (int a = 0; a < 2; ++a) {
          f32x4 v = acc[T][a] + bb;
#pragma unroll
          for (int i = 0; i < 4; ++i) v[i] = v[i] / (1.0f + __expf(-v[i]));
          if (h < 3 || T >= 8) st16c(__builtin_bit_cast(u32x4, v), c.ws, l16, faro(c, F_G + ((s * 2 + a) * 40 + n0 / 16 + T) * 1024));
        }
      }
      signal_wg(c, C_CG + rb);
      continue;
    }
    WStream ws, nx;
    const unsigned pitch = type == 2 ? 3072 : 12288;
    ws.r = mk_rsrc(type == 0 ? p.Wv : type == 1 ? p.Wk : type == 2 ? p.w1 : p.Wq, D_ * pitch);
    pfmt_voff<false>(ws.v, w, lane, pitch); ws.s0 = h * 384; ws.step = 64 * pitch;
    nx.r = mk_rsrc(p.Wo, D_ * H_ * E_ * 2); ufmt_voff(nx.v, w, lane); nx.s0 = h * 192 * 1280 + u0 * 128; nx.step = 128;
    proj<false, 10, false>(c, acc, ws, F_XN + s * 2 * 20 * 1024, 20, nx, type == 0, l16);
    if (type == 0) {
      uphase(c, acc, nx, s, u0 * 4, h, l16, nu);
      signal_wg(c, C_CU + rb, nu);
    } else if (type == 1) {
#pragma unroll
      for (int a = 0; a < 2; ++a)
#pragma unroll
        for (int m = 0; m < 6; ++m) st16c(__builtin_bit_cast(u32x4, pack8(acc[2 * m][a], acc[2 * m + 1][a])), c.ws, l16, kxo(c, F_KX + ((s * 32 + h) * 12 + a * 6 + m) * 1024));
      signal_wg(c, C_FK + rb * 32 + h);
    } else if (type == 2) {
#pragma unroll
      for (int T = 0; T < 12; ++T) {
        const u32x2 bv = *(const u32x2*)(p.b1 + h * 192 + T * 16 + g * 4);
        const f32x4 bb = {b2f(bv[0] & 0xffffu), __uint_as_float(bv[0] & 0xffff0000u), b2f(bv[1] & 0xffffu), __uint_as_float(bv[1] & 0xffff0000u)};
#pragma unroll
        for (int a = 0; a < 2; ++a) {
          f32x4 v = acc[T][a] + bb;
#pragma unroll
          for (int i = 0; i < 4; ++i) { const float r = v[i] > 0.f ? v[i] : 0.f; v[i] = r * r; }
          acc[T][a] = v;
        }
      }
#pragma unroll
      for (int a = 0; a < 2; ++a)
#pragma unroll
        for (int m = 0; m < 6; ++m) st16c(__builtin_bit_cast(u32x4, pack8(acc[2 * m][a], acc[2 * m + 1][a])), c.ws, l16, faro(c, F_HF + ((s * 2 + a) * 48 + h * 6 + m) * 1024));
      signal_wg(c, C_F1 + rb);
    } else {
      attn_tail(c, acc, s, h, l16);
    }
  }
}

DI int ctx_init(Ctx& c, const Params& p, int nwg) {
  char* base = ws_base(p.ws);
  c.ws = mk_rsrc(base, WS_END);
  c.ctl = (gu32*)(base + WS_CTL);
  c.lds = 0u;
  c.lane = threadIdx.x & 63; c.wave = rfl(threadIdx.x >> 6);
  const unsigned pv = __hip_atomic_load(cw(c, c.lane < NPROBE ? C_PAR + c.lane : C_T0), RLX_AGENT);
  LAS unsigned* tk = (LAS unsigned*)(LDS_BYTES - 16);
  if (threadIdx.x == 0) {
    const bool near = xcc_id() < 4;
    const int t = (int)__hip_atomic_fetch_add(cw(c, near ? C_TKA : C_TKB), 1u, RLX_AGENT);
    *tk = (unsigned)(near ? t : nwg - 1 - t);
  }
  {
    const unsigned raw = (unsigned)__ballot(pv != 0);
    const int lo = min(max(c.lane - 3, 0), NPROBE - 7);
    c.par = (unsigned)__ballot(__builtin_popcount((raw >> lo) & 127u) >= 4);
  }
  c.t0 = __builtin_amdgcn_readlane(pv, 32);
  LBAR();
  const int r = (int)rfl(*tk);
  LBAR();
  return r;
}

__global__ void __launch_bounds__(512, 2) k_main(Params p) {
  extern __shared__ __attribute__((aligned(16))) char smem[];
  Ctx c;
  const int role = ctx_init(c, p, 256);
#define ROLE(call) do { asm volatile("v_mbcnt_lo_u32_b32 %0, -1, 0\n\tv_mbcnt_hi_u32_b32 %0, -1, %0" : "=v"(c.lane)); call; STAMP_DUMP(c, role); __builtin_amdgcn_endpgm(); } while (0)
  if (role < 32) { if (c.wave < 2) ROLE(s_wave<0>(p, c, role)); else if (c.wave == 2) ROLE(s_wave<1>(p, c, role)); else if (c.wave == 3) ROLE(s_wave<3>(p, c, role)); else ROLE(s_wave<2>(p, c, role)); }
  else if (role < 64) { if (c.wave < 4) ROLE(v_wave<true>(p, c, role - 32)); else ROLE(v_wave<false>(p, c, role - 32)); }
  else if (role < NCHAIN) ROLE(m_wave(p, c, role - 64));
  else ROLE(bulk(p, c, 0, N_ITEMS));
}

constexpr int PREP_GRID = 512;
__global__ void __launch_bounds__(256) k_prep(Params p) {
  __shared__ float red[2][4][16];
  char* base = ws_base(p.ws);
  const int b = blockIdx.x, tid = threadIdx.x, lane = tid & 63, w = tid >> 6;
  if (b < 128) {
    const int tok = lane & 15, g = lane >> 4;
    const u32x4* src = (const u32x4*)(p.x + (size_t)(b * 16 + tok) * D_ + w * 160 + g * 8);
    float v[40];
#pragma unroll
    for (int k = 0; k < 5; ++k) { u32x4 d = src[k * 4];
#pragma unroll
      for (int j = 0; j < 4; ++j) { v[k * 8 + 2 * j] = b2f(d[j] & 0xffffu); v[k * 8 + 2 * j + 1] = __uint_as_float(d[j] & 0xffff0000u); } }
    float s = 0.f;
#pragma unroll
    for (int i = 0; i < 40; ++i) s += v[i];
    s = x32sum(x16sum(s));
    if (lane < 16) red[0][w][lane] = s;
    __syncthreads();
    const float mean = (red[0][0][tok] + red[0][1][tok] + red[0][2][tok] + red[0][3][tok]) * (1.0f / D_);
    float q = 0.f;
#pragma unroll
    for (int i = 0; i < 40; ++i) { v[i] -= mean; q += v[i] * v[i]; }
    q = x32sum(x16sum(q));
    if (lane < 16) red[1][w][lane] = q;
    __syncthreads();
    const float var = (red[1][0][tok] + red[1][1][tok] + red[1][2][tok] + red[1][3][tok]) * (1.0f / D_);
    const float rs = 1.0f / sqrtf(var + 1e-5f);
#pragma unroll
    for (int k = 0; k < 5; ++k) { u32x4 d;
#pragma unroll
      for (int j = 0; j < 4; ++j) d[j] = pk2(v[k * 8 + 2 * j] * rs, v[k * 8 + 2 * j + 1] * rs);
      u32x4* dst = (u32x4*)(base + nmap(F_XN + (b * 20 + w * 5 + k) * 1024)) + lane;
      dst[0] = d; dst[512] = d; }
  } else if (b < 128 + NPROBE) {
    const int k = b - 128;
    if (w == 0) {
      rsrc_t r = mk_rsrc(base, WS_END);
      const unsigned o = k * (8 * MB) + 7 * MB;
      unsigned best[2] = {~0u, ~0u};
      for (int rep = 0; rep < PROBE_REPS; ++rep)
#pragma unroll
        for (int hh = 0; hh < 2; ++hh) {
          const unsigned long long t0 = __builtin_amdgcn_s_memtime();
          st4c(SENT, r, (lane & 31) * 4, o + hh * 8192); WAIT_V(0);
          best[hh] = min(best[hh], (unsigned)(__builtin_amdgcn_s_memtime() - t0));
        }
      if (lane == 0) ((unsigned*)(base + WS_CTL))[(C_PAR + k) * 32] = (unsigned)(best[1] < best[0]) ^ (unsigned)(xcc_id() >= 4) ^ 1u;
    }
  } else if (b < 161) {
    const int i = (b - 159) * 256 + tid;
    if (i < C_N) ((unsigned*)(base + WS_CTL))[i * 32] = i == C_T0 ? (unsigned)__builtin_amdgcn_s_memrealtime() : 0u;
  } else if (b < 171) {
    const int i = (b - 161) * 256 + tid, r = i / 320, cp = i % 320, ct = cp >> 3, q = cp & 7;
    unsigned v = ((const unsigned*)p.mem0)[i]; v = v < 0xFFFFFFFEu ? v : 0xFFFFFFFEu;
    const unsigned o = nmap(ct * 256 + (4 * (r >> 1) + 2 * (q & 1) + (r & 1)) * 16 + (q >> 1) * 4);
    *(unsigned*)(base + o) = v; *(unsigned*)(base + o + 8192) = v;
  } else {
    const u32x4 sv = {SENT, SENT, SENT, SENT};
    constexpr unsigned n1 = (4 * MB + 1280 * KB) / 16, n2 = n1 + 128 * KB / 16;
    for (unsigned i = (b - 171) * 256 + tid; i < n2; i += (PREP_GRID - 171) * 256) {
      const unsigned o = i < n1 ? i * 16 : 2 * N_TP + (i - n1) * 16;
      if (o < 32768 && (o >> 14) * 8192 + (o & 8191u) < MEM_BYTES) continue;
      *(u32x4*)(base + o) = sv;
    }
  }
}

extern "C" void kernel_launch(void* const* d_in, const int* in_sizes, int n_in, void* d_out, int out_size, void* d_ws, size_t ws_size, hipStream_t stream) {
  Params p;
  p.x = (const u16*)d_in[0]; p.mem0 = (const u16*)d_in[1]; p.Wq = (const u16*)d_in[2]; p.Wk = (const u16*)d_in[3]; p.Wv = (const u16*)d_in[4]; p.Wo = (const u16*)d_in[5];
  p.w1 = (const u16*)d_in[8]; p.b1 = (const u16*)d_in[9]; p.w2 = (const u16*)d_in[10]; p.b2 = (const u16*)d_in[11];
  p.out = (u16*)d_out; p.ws = (char*)d_ws;
  hipFuncSetAttribute((const void*)k_main, hipFuncAttributeMaxDynamicSharedMemorySize, LDS_BYTES);
  k_prep<<<PREP_GRID, 256, 0, stream>>>(p);
  k_main<<<256, 512, LDS_BYTES, stream>>>(p);
}
```

```cpp
#include <hip/hip_runtime.h>
#include <hip/hip_bf16.h>
#include <stdint.h>

#define DI __device__ __forceinline__
#define LAS __attribute__((address_space(3)))
#define GAS __attribute__((address_space(1)))
typedef unsigned short u16;
typedef __bf16 bf16x2 __attribute__((ext_vector_type(2)));
typedef __bf16 bf16x4 __attribute__((ext_vector_type(4)));
typedef __bf16 bf16x8 __attribute__((ext_vector_type(8)));
typedef __bf16 bf16x16 __attribute__((ext_vector_type(16)));
typedef float f32x2 __attribute__((ext_vector_type(2)));
typedef float f32x4 __attribute__((ext_vector_type(4)));
typedef unsigned u32x2 __attribute__((ext_vector_type(2)));
typedef unsigned u32x4 __attribute__((ext_vector_type(4)));
typedef __amdgpu_buffer_rsrc_t rsrc_t;
typedef GAS unsigned gu32;
#define RLX_AGENT __ATOMIC_RELAXED, __HIP_MEMORY_SCOPE_AGENT

constexpr int T_ = 2048, D_ = 640, H_ = 32, E_ = 192, F_ = 1536, NS_ = 64;
constexpr unsigned MB = 1u << 20, KB = 1u << 10;
constexpr unsigned N_XREC = 0;
constexpr unsigned N_UM = 2 * MB;
constexpr unsigned N_KM = 24 * MB;
constexpr unsigned N_KX = 80 * MB;
constexpr unsigned N_TP = 123 * MB;
constexpr unsigned F_UX = 0;
constexpr unsigned F_HF = 104 * MB;
constexpr unsigned F_G = 110 * MB;
constexpr unsigned F_PX = 115 * MB;
constexpr unsigned F_XN = 120 * MB;
constexpr unsigned WS_CTL = 250 * MB;
constexpr unsigned WS_END = 254 * MB;
constexpr int NPROBE = 31;
constexpr unsigned MEM_BYTES = 10240, PCM_OFF = 12288, PCX_OFF = 16384;
constexpr unsigned TA_OFF = 10240, TM_OFF = 0, TS_OFF = 512;
constexpr int VIS_S = 3;
constexpr int VIS_M = 3;
constexpr int PRE_M = 28;
constexpr int VIS_MAX = 16, ZE0 = 152, AE0 = 85, PE0 = 204, FIN_LEAD = 5;
constexpr int C_ABORT = 2, C_T0 = 4, C_RDY = 5, C_PRB = 6, C_TK = 8  , C_Q = 16  , C_X = 24  , C_FK = 32  , C_CU = 288  , C_PS = 296  , C_PV = 328  ,
              C_F1 = 360  , C_CG = 368  , C_CP = 376  , C_N = 440, C_PAR = 440  , C_HB = 472  ;
constexpr unsigned SENT = 0xFFFFFFFFu;
constexpr unsigned SPIN_CAP = 1u << 18;
constexpr int LDS_BYTES = 160 * 1024;
constexpr int NCHAIN = 104;

struct Params {
  const u16 *x, *mem0, *Wq, *Wk, *Wv, *Wo, *w1, *b1, *w2, *b2;
  u16* out; char* ws;
};

DI rsrc_t mk_rsrc(const void* p, unsigned bytes) { return __builtin_amdgcn_make_buffer_rsrc((void*)p, (short)0, (int)bytes, 0x00020000); }
DI char* ws_base(char* ws) { return (char*)(((uintptr_t)ws + (2 * MB - 1)) & ~(uintptr_t)(2 * MB - 1)); }
DI unsigned rfl(unsigned v) { return __builtin_amdgcn_readfirstlane(v); }
DI unsigned xcc_id() { return (unsigned)__builtin_amdgcn_s_getreg(20 | (3 << 11)) & 0xfu; }
DI unsigned nmap(unsigned o) { return ((o >> 13) << 14) | (o & 8191u); }
DI unsigned pk2(float a, float b) { f32x2 f = {a, b}; return __builtin_bit_cast(unsigned, __builtin_convertvector(f, bf16x2)); }
DI unsigned pk2s(float a, float b) { unsigned v = pk2(a, b); return v < 0xFFFFFFFEu ? v : 0xFFFFFFFEu; }
DI float b2f(unsigned v) { return __uint_as_float(v << 16); }
DI bf16x8 pack8(f32x4 a, f32x4 b) { u32x4 r = {pk2(a[0], a[1]), pk2(a[2], a[3]), pk2(b[0], b[1]), pk2(b[2], b[3])}; return __builtin_bit_cast(bf16x8, r); }
#define WAIT_V(n) asm volatile("s_waitcnt vmcnt(" #n ")" ::: "memory")
#define WAIT_L0() asm volatile("s_waitcnt lgkmcnt(0)" ::: "memory")
#define BAR() do { asm volatile("" ::: "memory"); __builtin_amdgcn_s_barrier(); asm volatile("" ::: "memory"); } while (0)
#define LBAR() do { WAIT_L0(); BAR(); } while (0)
#define MFMA(a, b, c) __builtin_amdgcn_mfma_f32_16x16x32_bf16((a), (b), (c), 0, 0, 0)
#define SMF(a, b, c, i) __builtin_amdgcn_smfmac_f32_16x16x64_bf16((a), (b), (c), (i), 0, 0)

DI void glds(rsrc_t r, unsigned voff, unsigned soff, unsigned ldsdst) {
  asm volatile("s_mov_b32 m0, %0\n\ts_nop 0\n\tbuffer_load_dwordx4 %1, %2, %3 offen lds" ::"s"(ldsdst), "v"(voff), "s"(r), "s"(soff) : "memory");
}
DI bf16x4 trd(unsigned a) { return __builtin_amdgcn_ds_read_tr16_b64_v4bf16((LAS bf16x4*)a); }
DI bf16x8 cat4(bf16x4 a, bf16x4 b) { return __builtin_shufflevector(a, b, 0, 1, 2, 3, 4, 5, 6, 7); }
DI bf16x16 cat8(bf16x8 a, bf16x8 b) { return __builtin_shufflevector(a, b, 0, 1, 2, 3, 4, 5, 6, 7, 8, 9, 10, 11, 12, 13, 14, 15); }
DI bf16x8 lo8(bf16x16 v) { return __builtin_shufflevector(v, v, 0, 1, 2, 3, 4, 5, 6, 7); }
DI bf16x8 hi8(bf16x16 v) { return __builtin_shufflevector(v, v, 8, 9, 10, 11, 12, 13, 14, 15); }
DI bf16x8 ldsr16(unsigned a) { return *(LAS bf16x8*)a; }
DI u32x4 ld16c(rsrc_t r, unsigned v, unsigned s) { return __builtin_amdgcn_raw_buffer_load_b128(r, v, s, 16); }
DI unsigned ld4c(rsrc_t r, unsigned v, unsigned s) { return __builtin_amdgcn_raw_buffer_load_b32(r, v, s, 16); }
DI void st16c(u32x4 d, rsrc_t r, unsigned v, unsigned s) { __builtin_amdgcn_raw_buffer_store_b128(d, r, v, s, 16); asm volatile("s_nop 1" ::"v"(d) : "memory"); }
DI void st4c(unsigned d, rsrc_t r, unsigned v, unsigned s) { __builtin_amdgcn_raw_buffer_store_b32(d, r, v, s, 16); }
DI unsigned umax3(unsigned a, unsigned b, unsigned c) { return max(max(a, b), c); }
DI unsigned umax4(unsigned m, u32x4 v) { return umax3(umax3(m, v[0], v[1]), v[2], v[3]); }
DI float x16max(float v) { u32x2 r = __builtin_amdgcn_permlane16_swap(__float_as_uint(v), __float_as_uint(v), false, false); return fmaxf(__uint_as_float(r[0]), __uint_as_float(r[1])); }
DI float x32max(float v) { u32x2 r = __builtin_amdgcn_permlane32_swap(__float_as_uint(v), __float_as_uint(v), false, false); return fmaxf(__uint_as_float(r[0]), __uint_as_float(r[1])); }
DI float x16sum(float v) { u32x2 r = __builtin_amdgcn_permlane16_swap(__float_as_uint(v), __float_as_uint(v), false, false); return __uint_as_float(r[0]) + __uint_as_float(r[1]); }
DI float x32sum(float v) { u32x2 r = __builtin_amdgcn_permlane32_swap(__float_as_uint(v), __float_as_uint(v), false, false); return __uint_as_float(r[0]) + __uint_as_float(r[1]); }
DI unsigned dpp_x1(unsigned v) { return __builtin_amdgcn_update_dpp(v, v, 0xB1, 0xf, 0xf, false); }

struct Ctx {
  rsrc_t ws; gu32* ctl; int lane, wave; unsigned par, t0;
};
DI int now(const Ctx& c) { return (int)(((unsigned)__builtin_amdgcn_s_memrealtime() - c.t0) & 0x7FFFFFFFu); }
DI void wait_until(const Ctx& c, int t) { for (int i = 0; i < 4096 && now(c) < t; ++i) {} }
DI int wave_max(int v) {
  unsigned u = (unsigned)v;
  u = max(u, (unsigned)__builtin_amdgcn_update_dpp(0, u, 0x111, 0xf, 0xf, false)); u = max(u, (unsigned)__builtin_amdgcn_update_dpp(0, u, 0x112, 0xf, 0xf, false));
  u = max(u, (unsigned)__builtin_amdgcn_update_dpp(0, u, 0x114, 0xf, 0xf, false)); u = max(u, (unsigned)__builtin_amdgcn_update_dpp(0, u, 0x118, 0xf, 0xf, false));
  return (int)max(max((unsigned)__builtin_amdgcn_readlane(u, 15), (unsigned)__builtin_amdgcn_readlane(u, 31)), max((unsigned)__builtin_amdgcn_readlane(u, 47), (unsigned)__builtin_amdgcn_readlane(u, 63)));
}
DI gu32* cw(const Ctx& c, int i) { return c.ctl + i * 32; }
DI bool aborted(const Ctx& c) { return rfl(__hip_atomic_load(cw(c, C_ABORT), RLX_AGENT)) != 0; }
DI void do_abort(const Ctx& c, unsigned code) { if (c.lane == 0) __hip_atomic_store(cw(c, C_ABORT), code, RLX_AGENT); }
DI bool spin_fail(const Ctx& c, unsigned& spins, unsigned code) {
  ++spins;
  if (spins > SPIN_CAP) { do_abort(c, code); return true; }
  return (spins & 255u) == 0u && aborted(c);
}
DI void wait_ge(const Ctx& c, int i, unsigned want, unsigned code, unsigned lazy = 0) {
  for (unsigned spins = 0;;) {
    const unsigned v = rfl(__hip_atomic_load(cw(c, i), RLX_AGENT));
    if (v >= want) break;
    if (lazy && v + lazy < want) __builtin_amdgcn_s_sleep(24); else __builtin_amdgcn_s_sleep(2);
    if (spin_fail(c, spins, code)) break;
  }
  asm volatile("" ::: "memory");
}
DI unsigned par_words(const Ctx& c) { return __hip_atomic_load(cw(c, c.lane < NPROBE ? C_PAR + c.lane : C_T0), RLX_AGENT); }
DI void par_set(Ctx& c, unsigned pv) {
  const unsigned raw = (unsigned)__ballot(pv != 0);
  const int lo = min(max(c.lane - 3, 0), NPROBE - 7);
  c.par = (unsigned)__ballot(__builtin_popcount((raw >> lo) & 127u) >= 4);
  c.t0 = __builtin_amdgcn_readlane(pv, 32);
}
DI void load_par(Ctx& c, int word, unsigned n) { wait_ge(c, word, n, 0x50); par_set(c, par_words(c)); }
DI unsigned nearo(const Ctx& c, unsigned o) { return nmap(o) | ((((c.par >> (o >> 22)) ^ (o >> 20)) & 1u) << 13); }
DI unsigned faro(const Ctx& c, unsigned o) { return nearo(c, o) ^ 8192u; }
DI unsigned xrec_soff(const Ctx& c, int s) { return nearo(c, N_XREC + (unsigned)s * 32768u); }
DI unsigned tp_soff(const Ctx& c, int s) { return nearo(c, N_TP + (unsigned)s * 1024u); }
DI unsigned um_soff(const Ctx& c, int s) { const int pg = s / 3; return nearo(c, N_UM + (unsigned)pg * MB + (unsigned)(s - pg * 3) * (320 * KB)); }
DI unsigned ux_soff(const Ctx& c, int s, int ct, int h) { return faro(c, F_UX + (unsigned)(s * 40 + ct) * 32768u) + nmap((unsigned)((h + 4 * ct) & 31) * 1024u); }
DI unsigned km_soff(const Ctx& c, int s, int h) { return nearo(c, N_KM + (unsigned)(s * 32 + h) * 4096u); }

template <bool PI> DI void pfmt_voff(unsigned (&v)[3], int wave, int lane, unsigned pitch) {
#pragma unroll
  for (int i = 0; i < 3; ++i) {
    unsigned n = (wave * 3 + i) * 64 + lane, row = n / 24, pos = n % 24;
    unsigned key = PI ? ((row >> 1) & 3) : (((row >> 1) & 1) | (((row >> 3) & 1) << 1));
    v[i] = row * pitch + ((((pos >> 1) ^ key) << 1) | (pos & 1)) * 16;
  }
}
DI void ufmt_voff(unsigned (&v)[3], int wave, int lane) {
#pragma unroll
  for (int i = 0; i < 3; ++i) {
    unsigned n = (wave * 3 + i) * 64 + lane, row = n >> 3, pos = n & 7;
    v[i] = row * 1280u + ((((pos >> 1) ^ ((row >> 1) & 3)) << 1) | (pos & 1)) * 16;
  }
}
DI void stage_load(rsrc_t r, const unsigned (&v)[3], unsigned soff, unsigned slot, int wave) {
#pragma unroll
  for (int i = 0; i < 3; ++i) glds(r, v[i], soff, slot + (unsigned)(wave * 3 + i) * 1024u);
}
DI unsigned lane_kx(int lane) { return (((lane >> 4) & 1) << 1) | ((lane >> 3) & 1); }
template <bool PI> DI unsigned pfmt_base(int lane, int T) { int g = lane >> 4, q = (lane >> 2) & 3; return (unsigned)(((PI ? 4 : 8) * g + q) * 384 + ((T ^ lane_kx(lane)) * 32) + (lane & 3) * 8); }
DI unsigned ufmt_base(int lane, int C) { int g = lane >> 4, q = (lane >> 2) & 3; return (unsigned)((4 * g + q) * 128 + ((C ^ lane_kx(lane)) * 32) + (lane & 3) * 8); }
template <bool PI> DI bf16x8 pfmt_frag(unsigned a, int ks) { return cat4(trd(a + ks * 32 * 384), trd(a + ks * 32 * 384 + (PI ? 16 : 4) * 384)); }
DI bf16x8 ufmt_frag(unsigned a, int ks) { return cat4(trd(a + ks * 32 * 128), trd(a + ks * 32 * 128 + 16 * 128)); }
DI bf16x16 pfmt_frag64(unsigned a) { return cat8(pfmt_frag<false>(a, 0), pfmt_frag<false>(a, 1)); }

constexpr unsigned L_MEM = 120 * KB, L_Q = 130 * KB, L_K = 136 * KB, L_KX = 142 * KB, L_MISC = 154 * KB;
DI constexpr unsigned slot_off(int i) { return i == 0 ? 0u : i == 1 ? 72 * KB : i == 2 ? 24 * KB : i == 3 ? 120 * KB : i == 4 ? 48 * KB : 96 * KB; }
#define OPQ(x) asm volatile("" : "+v"(x))
#define SB() __builtin_amdgcn_sched_barrier(0)


#define POLL(c, bud, code, ...) do { bool ok; \
    _Pragma("nounroll") do { asm volatile("" ::: "memory"); __VA_ARGS__; } while (!ok && --(bud) > 0); \
    if (__builtin_expect(!ok, 0)) do_abort(c, code); } while (0)
DI void poll_mem(const Ctx& c, int& bud, int s, unsigned l16, int u, unsigned la) {
  const unsigned so = xrec_soff(c, s) + u * 1024;
  la += u * 1024;
  if (u < 2) {
    POLL(c, bud, 0x100 + s, { u32x4 a = ld16c(c.ws, l16, so); u32x4 b = ld16c(c.ws, l16, so + 4096); u32x4 d = ld16c(c.ws, l16, so + 16384);
      *(LAS u32x4*)(la) = a; *(LAS u32x4*)(la + 4096) = b; *(LAS u32x4*)(la + 8192) = d;
      ok = __all(umax4(umax4(umax4(0u, a), b), d) != SENT); });
  } else {
    POLL(c, bud, 0x100 + s, { u32x4 a = ld16c(c.ws, l16, so); u32x4 b = ld16c(c.ws, l16, so + 4096);
      *(LAS u32x4*)(la) = a; *(LAS u32x4*)(la + 4096) = b;
      ok = __all(umax4(umax4(0u, a), b) != SENT); });
  }
}
DI int last_pub(const Ctx& c, int s, unsigned off, unsigned n, unsigned l16) { OPQ(l16); return wave_max((int)ld4c(c.ws, min(l16 >> 2, (n - 1) * 4), tp_soff(c, s) + off)); }
DI int last_arr(const Ctx& c, int s, unsigned l16) { OPQ(l16); return wave_max((int)ld4c(c.ws, (l16 << 2) & 2047u, xrec_soff(c, s) + nmap(TA_OFF))); }
DI int wait_arr(const Ctx& c, int s, unsigned l16) {
  int t = last_arr(c, s, l16);
  for (int i = 0; i < 64 && t < 0; ++i) { asm volatile("" ::: "memory"); __builtin_amdgcn_s_sleep(2); t = last_arr(c, s, l16); }
  return t;
}
DI int track(int est, int obs) { return max(min(obs, est + 1), est - 2); }
DI int pace_sv(const Ctx& c, int s, int& ta0, int& ze, unsigned l16, int& vis, int missed) {
  if (s + 1 >= NS_) return 0;
  OPQ(l16);
  const unsigned vm = ld4c(c.ws, min(l16 >> 2, 156u), tp_soff(c, s) + TM_OFF), va = ld4c(c.ws, (l16 << 2) & 2047u, xrec_soff(c, s) + nmap(TA_OFF));
  const int tm = wave_max((int)vm);
  if (tm >= 0 && ta0 >= 0) ze = track(ze, tm - ta0);
  if (tm >= 0 && missed >= tm - 2) vis = min(vis + 1, VIS_MAX);
  ta0 = wave_max((int)va);
  if (ta0 < 0) ta0 = wait_arr(c, s, l16);
  return ta0 >= 0 && ze < 400 ? ta0 + ze + vis : 0;
}
DI bf16x16 ldsr32(unsigned a) { return cat8(ldsr16(a), ldsr16(a + 1024)); }
DI void ldsw32(unsigned a, bf16x16 v) {
  *(LAS bf16x8*)a = __builtin_shufflevector(v, v, 0, 1, 2, 3, 4, 5, 6, 7); *(LAS bf16x8*)(a + 1024) = __builtin_shufflevector(v, v, 8, 9, 10, 11, 12, 13, 14, 15);
}
DI f32x2 hsum(f32x4 v) { float a = v[0] + v[2], b = v[1] + v[3]; OPQ(a); OPQ(b); return f32x2{a, b}; }
DI f32x2 smul(float a, float b, float k) { a *= k; b *= k; OPQ(a); OPQ(b); return f32x2{a, b}; }
DI float x2(float v) { return __uint_as_float(__builtin_amdgcn_update_dpp(0u, __float_as_uint(v), 0x4E, 0xf, 0xf, true)); }

template <int KIND> DI void s_wave(const Params& p, Ctx c, int h) {
  constexpr bool QW = KIND != 2;
  constexpr int NR = QW ? 7 : 8;
  constexpr unsigned LB = QW ? 0 : 72 * KB;
  const int lane = c.lane, w = c.wave, u = w & 3;
  bf16x16 W[NR][3];
  unsigned wl = LB + u * 6144 + lane * 16; OPQ(wl);
  {
    const unsigned tl = pfmt_base<false>(lane, u);
    rsrc_t rq = mk_rsrc(p.Wq, D_ * H_ * E_ * 2), rk = mk_rsrc(p.Wk, D_ * H_ * E_ * 2);
    unsigned v[3]; pfmt_voff<false>(v, w, lane, 12288);
    const unsigned s0 = h * 384;
#pragma unroll
    for (int i = 0; i < 2; ++i) { stage_load(rq, v, s0 + i * 64 * 12288, slot_off(2 * i), w); stage_load(rk, v, s0 + i * 64 * 12288, slot_off(2 * i + 1), w); }
#pragma unroll
    for (int i = 0; i < 10; ++i) {
      if (i < 9) WAIT_V(6); else WAIT_V(0);
      LBAR();
      if (i + 2 < 10) { stage_load(rq, v, s0 + (i + 2) * 64 * 12288, slot_off(2 * ((i + 2) % 3)), w); stage_load(rk, v, s0 + (i + 2) * 64 * 12288, slot_off(2 * ((i + 2) % 3) + 1), w); }
      const unsigned so = slot_off(2 * (i % 3) + (QW ? 0 : 1));
      bf16x16 f[3];
#pragma unroll
      for (int t = 0; t < 3; ++t) f[t] = pfmt_frag64(tl + t * 128 + so);
      if (i >= 7) LBAR();
#pragma unroll
      for (int t = 0; t < 3; ++t) { if (i < NR) W[i < NR ? i : 0][t] = f[t]; else ldsw32(wl + so - LB + t * 2048, f[t]); }
    }
    LBAR();
  }
  load_par(c, C_RDY, 24);
  const int idx = (lane & 2) ? 0xEEEE : 0x4444;
  int bud = 1 << 20;
  const int g = lane >> 4, r16 = lane & 15;
  unsigned l16 = lane * 16; OPQ(l16); l16 &= 0x3F0u;
  unsigned memr = L_MEM + lane * 16; OPQ(memr);
  unsigned qkw = QW ? L_Q + (16 * u + r16) * 32 + ((g ^ (r16 >> 2)) * 8)
                    : L_K + (16 * ((r16 >> 2) & 1) + 4 * ((u & 2) + (r16 >> 3)) + 2 * (u & 1) + (r16 & 1)) * 32 + g * 8 + (r16 & 2) * 2;
  OPQ(qkw);
  unsigned kr = L_K + (4 * g + (r16 >> 2)) * 32 + (lane & 3) * 8;
  unsigned qkr = L_Q + (4 * g + (r16 >> 2)) * 32 + (((lane & 3) ^ g) * 8);
  unsigned kxa = L_KX + ((r16 >> 2) & 1) * 6144 + (g * 16 + 8 * u + 4 * (r16 >> 3) + (r16 & 3)) * 16;
  unsigned pxo = nmap(PCX_OFF + h * 512) + u * 256 + r16 * 16 + g * 4, pmo = nmap(PCM_OFF + (h >> 1) * 256) + ((r16 & 13) | ((g & 1) << 1)) * 16 + (h & 1) * 8 + (g >> 1) * 4;
  unsigned exw = L_MISC + u * 1024 + lane * 16;
  if (KIND < 2) OPQ(kr);
  if (KIND == 0) { OPQ(qkr); OPQ(kxa); OPQ(pxo); OPQ(pmo); OPQ(exw); }
  unsigned qfl = L_MISC + 2048, qfw = qfl + u; if (QW) OPQ(qfw);
  if (KIND == 0) OPQ(qfl);
  if (w == 0) *(volatile LAS unsigned*)(qfl) = 0;
  const unsigned kxw = memr + (L_KX - L_MEM);
  if (KIND == 3) {
    wait_ge(c, C_FK + h, 1, 0x200);
#pragma unroll
    for (int i = 0; i < 12; ++i) *(LAS u32x4*)(kxw + i * 1024) = ld16c(c.ws, l16, nearo(c, N_KX + (h * 12 + i) * 1024));
  }
  const bool hi2 = lane & 2;
  int shot = 0, ref0 = -1, y0p = ZE0, vis = VIS_S;
  const float sc = 0.07216878364870322f * 1.4426950408889634f;
  for (int s = 0; s < NS_; ++s) {
    bf16x16 bt[3];
#pragma unroll
    for (int t = 0; t < 3; ++t) bt[t] = ldsr32(wl + slot_off(2 * (NR % 3) + (QW ? 0 : 1)) - LB + t * 2048);
    unsigned sv1 = s + 1; if (QW) OPQ(sv1);
    f32x4 acc[3] = {};
    OPQ(acc[0]); OPQ(acc[1]); OPQ(acc[2]);
    unsigned so = xrec_soff(c, s); asm volatile("" : "+s"(so));
    unsigned kb[3];
    if (KIND == 3) {
      const int sn = s + 1 < NS_ ? s + 1 : s;
#pragma unroll
      for (int j = 0; j < 3; ++j) { kb[j] = nearo(c, N_KX + (sn * 32 + h) * 12288 + j * 4096); asm volatile("" : "+s"(kb[j])); }
    }
    SB();
    const int bud0 = bud;
    if (!QW) { wait_until(c, shot); poll_mem(c, bud, s, l16, u, memr); }
    LBAR();
    if (KIND == 2 && w == 7) st4c((unsigned)now(c), c.ws, 0, so + nmap(TA_OFF) + h * 64);
    {
      constexpr int ord[10] = {NR, 0, 1, 2, QW ? 8 : 3, QW ? 3 : 4, QW ? 4 : 5, QW ? 5 : 6, QW ? 6 : 7, 9};
      constexpr int AWIN = QW ? 4 : 3;
      bf16x8 a[10];
#pragma unroll
      for (int i = 0; i < AWIN; ++i) { a[i] = ldsr16(memr + ord[i] * 1024); SB(); }
#pragma unroll
      for (int i = 0; i < 10; ++i) {
        const int ks = ord[i];
#pragma unroll
        for (int t = 0; t < 3; ++t) {
          acc[t] = SMF(a[i], ks < NR ? W[ks < NR ? ks : 0][t] : bt[t], acc[t], idx);
          OPQ(acc[t]);
          SB();
          if (ks >= NR && ks < 9) { bt[t] = ldsr32(wl + slot_off(2 * ((ks + 1) % 3) + (QW ? 0 : 1)) - LB + t * 2048); SB(); }
        }
        if (i + AWIN < 10) { a[i + AWIN] = ldsr16(memr + ord[i + AWIN] * 1024); SB(); }
      }
    }
#pragma unroll
    for (int t = 0; t < 3; ++t) { const f32x2 r = hsum(acc[t]); const unsigned d = pk2(r[0], r[1]); if (QW) *(LAS u32x2*)(qkw + t * 2048) = u32x2{d, d}; else *(LAS unsigned*)(qkw + t * 1024) = d; }
    if (QW) { asm volatile("" ::: "memory"); *(volatile LAS unsigned char*)(qfw) = (unsigned char)sv1; }
    bf16x16 qf[3]; f32x2 y, ex; float lx; f32x4 sm;
    u32x4 kxn[12]; unsigned fk;
    if (KIND == 0) {
      bf16x8 kx[6];
      SB();
#pragma unroll
      for (int m = 0; m < 6; ++m) kx[m] = ldsr16(kxa + m * 1024);
      SB();
      for (int sp = 1 << 22; --sp;) { if (rfl(*(volatile LAS unsigned*)(qfl)) == (unsigned)(s + 1) * 0x01010101u) break; }
      asm volatile("" ::: "memory");
#pragma unroll
      for (int m = 0; m < 3; ++m) qf[m] = cat8(cat4(trd(qkr + m * 2048), trd(qkr + m * 2048 + 512)), cat4(trd(qkr + m * 2048 + 1024), trd(qkr + m * 2048 + 1536)));
      SB();
      f32x4 sx = {};
#pragma unroll
      for (int m = 0; m < 6; ++m) sx = MFMA(kx[m], (m & 1) ? hi8(qf[m >> 1]) : lo8(qf[m >> 1]), sx);
      OPQ(sx);
      y = smul(hi2 ? sx[2] : sx[0], hi2 ? sx[3] : sx[1], sc);
      ex = f32x2{__builtin_amdgcn_exp2f(y[0]), __builtin_amdgcn_exp2f(y[1])};
      lx = ex[0] + ex[1];
      *(LAS f32x4*)exw = f32x4{lx, y[0], y[1], lx};
      sm = f32x4{}; OPQ(sm);
    }
    if (KIND == 3) {
      OPQ(l16); __builtin_assume(l16 < 1024u);
#pragma unroll
      for (int i = 0; i < 12; ++i) kxn[i] = ld16c(c.ws, l16 + (i & 3) * 1024, kb[i >> 2]);
      fk = (s & 7) == 6 ? __hip_atomic_load(cw(c, C_FK + min((s >> 3) + 1, 7) * 32 + h), RLX_AGENT) : 0u;
    }
    LBAR();
    if (!QW) shot = pace_sv(c, s, ref0, y0p, l16, vis, bud != bud0 ? shot : -1);
    if (KIND == 0) {
      if (s + 1 < NS_) {
        bf16x8 kf[3];
#pragma unroll
        for (int m = 0; m < 3; ++m) { kf[m] = cat4(trd(kr + m * 1024), trd(kr + m * 1024 + 512)); SB(); }
        const float lo = *(LAS float*)(exw ^ 1024);
        SB();
#pragma unroll
        for (int m = 0; m < 3; ++m) { sm = SMF(kf[m], qf[m], sm, idx); OPQ(sm); SB(); }
        lx += lo; lx += x2(lx); OPQ(lx); SB();
        const f32x2 zh = hsum(sm), z = smul(zh[0], zh[1], sc);
        f32x2 em = {__builtin_amdgcn_exp2f(z[0]), __builtin_amdgcn_exp2f(z[1])};
        float l = x32sum(x16sum(lx + (em[0] + em[1])));
        if (__builtin_expect(!__all(__float_as_uint(l) - 0x21800000u < 0x50000000u), 0)) {
          const f32x4 o = *(LAS f32x4*)(exw ^ 1024);
          float mx = fmaxf(fmaxf(fmaxf(y[0], y[1]), fmaxf(o[1], o[2])), fmaxf(z[0], z[1]));
          mx = fmaxf(mx, x2(mx)); mx = x32max(x16max(mx));
          ex[0] = __builtin_amdgcn_exp2f(y[0] - mx); ex[1] = __builtin_amdgcn_exp2f(y[1] - mx);
          em[0] = __builtin_amdgcn_exp2f(z[0] - mx); em[1] = __builtin_amdgcn_exp2f(z[1] - mx);
          l = ((ex[0] + ex[1]) + (__builtin_amdgcn_exp2f(o[1] - mx) + __builtin_amdgcn_exp2f(o[2] - mx))) + 0.5f * (em[0] + em[1]);
          l += x2(l); l = x32sum(x16sum(l));
          if (!(l == l)) { l = 1.0f; ex[0] = ex[1] = em[0] = em[1] = __uint_as_float(0x7FC00000u); }
        }
        const float inv = __builtin_amdgcn_rcpf(l);
        ex = smul(ex[0], ex[1], inv);
        st4c(pk2(ex[0], ex[1]), c.ws, pxo, so);
        if (u == 1) {
          em = smul(em[0], em[1], inv);
          st4c(pk2(em[0], em[1]), c.ws, pmo, so);
          st4c((unsigned)now(c), c.ws, 0, tp_soff(c, s) + TS_OFF + h * 4);
        }
      }
    }
    if (KIND == 1) {
      if (s + 1 < NS_) __builtin_amdgcn_s_sleep(4);
#pragma unroll
      for (int m = 0; m < 3; ++m) st16c(__builtin_bit_cast(u32x4, cat4(trd(kr + m * 1024), trd(kr + m * 1024 + 512))), c.ws, l16, km_soff(c, s, h) + m * 1024);
      WAIT_V(0);
      if (lane == 0) __hip_atomic_store(cw(c, C_PS + h), (unsigned)(s + 1), RLX_AGENT);
    }
    if (KIND == 3) {
      __builtin_amdgcn_s_sleep(4);
#pragma unroll
      for (int i = 0; i < 12; ++i) *(LAS u32x4*)(kxw + i * 1024) = kxn[i];
      if (((s + 2) & 7) == 0 && s + 2 < NS_ && rfl(fk) == 0) wait_ge(c, C_FK + ((s + 2) >> 3) * 32 + h, 1, 0x201);
      asm volatile("" ::"v"(fk) : "memory");
    }
  }
}

template <bool AW> DI void v_wave(const Params& p, Ctx c, int h) {
  constexpr int N1 = AW ? 2 : 1, N2R = AW ? 2 : 5;
  const int lane = c.lane, w = c.wave, u = w & 3;
  bf16x16 W1[8][N1], W2[N2R][3];
  unsigned w1l = 72 * KB + (AW ? u * 4096 : 16384 + u * 2048) + lane * 16; OPQ(w1l);
  unsigned w2l = u * 6144 + lane * 16;
  if (AW) OPQ(w2l);
  {
    const unsigned tl = pfmt_base<false>(lane, AW ? u : u + 8);
    const int gb = lane >> 4, qq = (lane >> 2) & 3;
    const unsigned ur = (32 * (gb & 1) + 8 * (gb >> 1) + 2 * (qq & 1) + (qq >> 1)) * 128 + (lane & 3) * 8;
    const unsigned ul0 = ur + ((u ^ (qq & 1)) * 32), ul1 = ur + ((u ^ (2 + (qq & 1))) * 32) + 4 * 128;
    rsrc_t rv = mk_rsrc(p.Wv, D_ * H_ * E_ * 2), ro = mk_rsrc(p.Wo, D_ * H_ * E_ * 2);
    unsigned v[3], vu[3]; pfmt_voff<false>(v, w, lane, 12288); ufmt_voff(vu, w, lane);
    const unsigned s0 = h * 384, u0 = h * 192 * 1280;
#pragma unroll
    for (int i = 0; i < 2; ++i) { stage_load(ro, vu, u0 + i * 128, slot_off(2 * i), w); stage_load(rv, v, s0 + i * 64 * 12288, slot_off(2 * i + 1), w); }
#pragma unroll
    for (int i = 0; i < 10; ++i) {
      if (i < 9) WAIT_V(6); else WAIT_V(0);
      LBAR();
      if (i + 2 < 10) { stage_load(ro, vu, u0 + (i + 2) * 128, slot_off(2 * ((i + 2) % 3)), w); stage_load(rv, v, s0 + (i + 2) * 64 * 12288, slot_off(2 * ((i + 2) % 3) + 1), w); }
      bf16x16 f1[N1], f2[3];
      const bool mine = AW ? i >= 5 : i < 5;
#pragma unroll
      for (int t = 0; t < N1; ++t) f1[t] = pfmt_frag64(tl + t * 128 + slot_off(2 * (i % 3) + 1));
      if (mine) {
#pragma unroll
        for (int kk = 0; kk < 3; ++kk) { const unsigned o = slot_off(2 * (i % 3)) + kk * 64 * 128; f2[kk] = cat8(cat4(trd(ul0 + o), trd(ul1 + o)), cat4(trd(ul0 + o + 16 * 128), trd(ul1 + o + 16 * 128))); }
      }
      if (i >= 7) LBAR();
#pragma unroll
      for (int t = 0; t < N1; ++t) { if (i < 8) W1[i < 8 ? i : 0][t] = f1[t]; else ldsw32(w1l + slot_off(2 * (i % 3) + 1) - 72 * KB + t * 2048, f1[t]); }
      if (mine) {
#pragma unroll
        for (int kk = 0; kk < 3; ++kk) { if (i < 7) W2[AW ? (i == 5 ? 0 : 1) : (i < 5 ? i : 0)][kk] = f2[kk]; else ldsw32(w2l + slot_off(2 * (i % 3)) + kk * 2048, f2[kk]); }
      }
    }
    LBAR();
  }
  load_par(c, C_RDY, 24);
  const int idx = (lane & 2) ? 0xEEEE : 0x4444;
  int bud = 1 << 20;
  const int g = lane >> 4, r16 = lane & 15;
  unsigned l16 = lane * 16; OPQ(l16); l16 &= 0x3F0u;
  unsigned memr = L_MEM + lane * 16; OPQ(memr);
  unsigned vw = L_Q + ((AW ? u : u + 8) * 8 + (r16 >> 1)) * 32 + g * 8 + (r16 & 1) * 4; OPQ(vw);
  unsigned vr = L_Q + (4 * g + (r16 >> 2)) * 32 + (lane & 3) * 8; OPQ(vr);
  unsigned stv = h * 256 + r16 * 16 + g * 65536 + u * 16384 + (AW ? 5 * 65536 : 0); OPQ(stv);
  unsigned st1 = h * 256 + r16 * 16 + g * 4 + u * 16384 + (AW ? 9 * 65536 : 4 * 65536); OPQ(st1);
  int shot = 0, tm0 = -1, y0p = ZE0, vis = VIS_S;
  for (int s = 0; s < NS_; ++s) {
    bf16x16 bt[N1];
#pragma unroll
    for (int t = 0; t < N1; ++t) bt[t] = ldsr32(w1l + slot_off(5) - 72 * KB + t * 2048);
    f32x4 acc[N1] = {};
#pragma unroll
    for (int t = 0; t < N1; ++t) OPQ(acc[t]);
    unsigned so = um_soff(c, s); asm volatile("" : "+s"(so));
    f32x4 oz[5] = {};
#pragma unroll
    for (int j = 0; j < 5; ++j) OPQ(oz[j]);
    SB();
    const int bud0 = bud;
    if (!AW) { wait_until(c, shot); poll_mem(c, bud, s, l16, u, memr); }
    WAIT_V(0);
    LBAR();
    {
      constexpr int ord[10] = {8, 0, 1, 2, 3, 4, 5, 6, 7, 9};
      constexpr int AWIN = AW ? 4 : 6;
      bf16x8 a[10];
#pragma unroll
      for (int i = 0; i < AWIN; ++i) { a[i] = ldsr16(memr + ord[i] * 1024); SB(); }
#pragma unroll
      for (int i = 0; i < 10; ++i) {
        const int ks = ord[i];
#pragma unroll
        for (int t = 0; t < N1; ++t) {
          acc[t] = SMF(a[i], ks < 8 ? W1[ks < 8 ? ks : 0][t] : bt[t], acc[t], idx);
          OPQ(acc[t]); SB();
          if (ks == 8) { bt[t] = ldsr32(w1l + slot_off(1) - 72 * KB + t * 2048); SB(); }
        }
        if (i + AWIN < 10) { a[i + AWIN] = ldsr16(memr + ord[i + AWIN] * 1024); SB(); }
      }
    }
#pragma unroll
    for (int t = 0; t < N1; ++t) { const f32x2 r = hsum(acc[t]); *(LAS unsigned*)(vw + t * 1024) = pk2(r[0], r[1]); }
    SB();
    bf16x16 ut[3];
    if (AW) {
#pragma unroll
      for (int kk = 0; kk < 3; ++kk) ut[kk] = ldsr32(w2l + slot_off(2) + kk * 2048);
    }
    if (AW && w == 0 && lane == 0 && s > 0) __hip_atomic_store(cw(c, C_PV + h), (unsigned)s, RLX_AGENT);
    LBAR();
    bf16x8 va[3];
#pragma unroll
    for (int kk = 0; kk < 3; ++kk) { va[kk] = cat4(trd(vr + kk * 1024), trd(vr + kk * 1024 + 512)); SB(); }
    SB();
    auto tile = [&](const bf16x16 (&b)[3], int j) { f32x4 o = oz[j];
#pragma unroll
      for (int kk = 0; kk < 3; ++kk) o = SMF(va[kk], b[kk], o, idx);
      OPQ(o); SB();
      return o; };
    auto refill = [&](unsigned off) {
#pragma unroll
      for (int kk = 0; kk < 3; ++kk) ut[kk] = ldsr32(w2l + off + kk * 2048);
      SB(); };
    f32x4 o[5];
    if (AW) {
      o[2] = tile(ut, 0); refill(slot_off(4));
      o[0] = tile(W2[0], 1);
      o[3] = tile(ut, 3); refill(slot_off(0));
      o[1] = tile(W2[1], 2);
      o[4] = tile(ut, 4);
    } else {
#pragma unroll
      for (int j = 0; j < 5; ++j) o[j] = tile(W2[AW ? 0 : j], j);
    }
    f32x2 r[5]; unsigned d[5];
#pragma unroll
    for (int j = 0; j < 5; ++j) r[j] = hsum(o[j]);
#pragma unroll
    for (int j = 0; j < 5; ++j) d[j] = pk2s(r[j][0], r[j][1]);
    {
      const u32x2 x = __builtin_amdgcn_permlane16_swap(d[0], d[1], false, false), y = __builtin_amdgcn_permlane16_swap(d[2], d[3], false, false);
      const u32x2 e = __builtin_amdgcn_permlane32_swap(x[0], y[0], false, false), f = __builtin_amdgcn_permlane32_swap(x[1], y[1], false, false);
      st16c(u32x4{e[0], f[0], e[1], f[1]}, c.ws, stv, so);
    }
    st4c(d[4], c.ws, st1, so);
    if (!AW) shot = pace_sv(c, s, tm0, y0p, l16, vis, bud != bud0 ? shot : -1);
    if (AW && s + 2 < NS_) {
      __builtin_amdgcn_s_sleep(24);
      const unsigned sr = um_soff(c, s + 2) + h * 256 + u * 16384; const u32x4 d = {SENT, SENT, SENT, SENT};
      unsigned rv = l16; OPQ(rv); rv = (rv & 255u) + (rv >> 8) * 65536;
      st16c(d, c.ws, rv, sr); st16c(d, c.ws, rv, sr + 4 * 65536); if (lane < 32) st16c(d, c.ws, rv, sr + 8 * 65536);
    }
  }
  WAIT_V(0);
  BAR();
  if (w == 0 && lane == 0) __hip_atomic_store(cw(c, C_PV + h), (unsigned)NS_, RLX_AGENT);
}

DI void m_wave(const Params& p, Ctx c, int ct) {
  const int lane = c.lane, w = c.wave;
  load_par(c, C_RDY, 24);
  const int idx = (lane & 2) ? 0xEEEE : 0x4444;
  int bud = 1 << 20;
  const int g = lane >> 4, r16 = lane & 15;
  unsigned l16 = lane * 16; OPQ(l16); l16 &= 0x3F0u;
  unsigned vpub = nmap(ct * 256) + (4 * g + (r16 & 3)) * 16 + (r16 >> 2) * 4; OPQ(vpub);
  unsigned zr = 64; OPQ(zr);
  volatile LAS unsigned* done = (volatile LAS unsigned*)(zr);
  if (w == 0) { done[0] = 0; done[4] = 0; done[5] = VIS_M; done[1] = ~0u; done[2] = AE0; done[3] = PE0; }
  LBAR();
  for (int s = w < 2 ? w : NS_; s < NS_ - 1; s += 2) {
    u32x4 bx[32], bm[8];
    wait_ge(c, C_CU + (s >> 3), 320, 0x300, 120);
    for (int sp = 0; rfl(done[4]) < (unsigned)s && sp < (1 << 24); ++sp) __builtin_amdgcn_s_sleep(1);
#pragma unroll
    for (int i = 0; i < 16; ++i) bx[i] = ld16c(c.ws, l16, ux_soff(c, s, ct, i & ~3) + (i & 3) * 1024);
    SB();
    for (int sp = 0; rfl(done[0]) < (unsigned)s && sp < (1 << 24); ++sp) __builtin_amdgcn_s_sleep(1);
#pragma unroll
    for (int i = 16; i < 32; ++i) bx[i] = ld16c(c.ws, l16, ux_soff(c, s, ct, i & ~3) + (i & 3) * 1024);
    SB();
    unsigned so = xrec_soff(c, s), su = um_soff(c, s) + ct * 16384, sn = xrec_soff(c, s + 1), sm = so + nmap(PCM_OFF);
    unsigned sx[4] = {so + nmap(PCX_OFF), so + nmap(PCX_OFF + 4096), so + nmap(PCX_OFF + 8192), so + nmap(PCX_OFF + 12288)}, su2 = su + 4096;
    asm volatile("" : "+s"(sm), "+s"(su), "+s"(sn), "+s"(su2), "+s"(sx[0]), "+s"(sx[1]), "+s"(sx[2]), "+s"(sx[3]));
    const int vis = (int)rfl(done[5]), bud0 = bud;
    if (s > 0) {
      int ta1 = (int)rfl(done[1]), ae = (int)rfl(done[2]), pe = (int)rfl(done[3]);
      const int ts1 = last_pub(c, s - 1, TS_OFF, 32, l16);
      if (ts1 >= 0 && ta1 >= 0) ae = track(ae, ts1 - ta1);
      if (ta1 >= 0 && pe < 400) wait_until(c, ta1 + pe + PRE_M);
      const int ta = wait_arr(c, s, l16);
      if (ta >= 0 && ta1 >= 0) pe = track(pe, ta - ta1);
      done[1] = (unsigned)ta; done[2] = (unsigned)ae; done[3] = (unsigned)pe;
      if (ta >= 0 && ae < 300) wait_until(c, ta + ae + vis);
    }
    unsigned res;
    POLL(c, bud, 0x500 + s, {
      u32x4 ax[16], am[4];
      OPQ(l16); __builtin_assume(l16 < 1024u);
      _Pragma("unroll") for (int i = 0; i < 16; ++i) { ax[i] = ld16c(c.ws, l16 + (i & 3) * 1024, sx[i >> 2]); SB(); }
      _Pragma("unroll") for (int i = 0; i < 4; ++i) { am[i] = ld16c(c.ws, l16 + i * 1024, sm); SB(); }
      _Pragma("unroll") for (int i = 0; i < 8; ++i) { bm[i] = ld16c(c.ws, l16 + (i & 3) * 1024, i < 4 ? su : su2); SB(); }
      done[4] = (unsigned)(s + 1);
      f32x2 zl, zh; asm volatile("v_mov_b64 %0, 0\n\tv_mov_b64 %1, 0\n\ts_nop 1" : "=v"(zl), "=v"(zh));
      f32x4 acc = __builtin_shufflevector(zl, zh, 0, 1, 2, 3);
      SB();
      _Pragma("unroll") for (int i = 0; i < 16; ++i) acc = SMF(__builtin_bit_cast(bf16x8, ax[i]), cat8(__builtin_bit_cast(bf16x8, bx[2 * i]), __builtin_bit_cast(bf16x8, bx[2 * i + 1])), acc, idx);
      _Pragma("unroll") for (int i = 0; i < 4; ++i) {
        acc = SMF(__builtin_bit_cast(bf16x8, am[i]), cat8(__builtin_bit_cast(bf16x8, bm[2 * i]), __builtin_bit_cast(bf16x8, bm[2 * i + 1])), acc, idx); }
      const f32x2 hs = hsum(acc); const float s0 = hs[0]; const float s1 = hs[1];
      const bool odd = lane & 1;
      const float t = __uint_as_float(dpp_x1(__float_as_uint(odd ? s0 : s1)));
      res = pk2s(odd ? t : s0, odd ? s1 : t); OPQ(res);
      ok = __all(s0 == s0 && s1 == s1);
      if (!ok) { unsigned m = 0;
        _Pragma("unroll") for (int i = 0; i < 16; ++i) m = umax4(m, ax[i]);
        _Pragma("unroll") for (int i = 0; i < 8; ++i) m = umax4(umax4(m, am[i & 3]), bm[i]);
        ok = __all(m != SENT); } });
    st4c(res, c.ws, vpub, sn);
    st4c((unsigned)now(c), c.ws, 0, tp_soff(c, s + 1) + TM_OFF + ct * 4);
    if (bud != bud0 && s > 0) done[5] = (unsigned)min(vis + 1, VIS_MAX);
    done[0] = (unsigned)(s + 1);
  }
}

template <int OFF> DI void xload(u32x4& d, rsrc_t r, unsigned v, unsigned s) {
  asm volatile("buffer_load_dwordx4 %0, %1, %2, %3 offen offset:%4 sc1" : "=v"(d) : "v"(v), "s"(r), "s"(s), "i"(OFF) : "memory");
}
#define XPIN(a, b) asm volatile("" : "+v"(a), "+v"(b))
constexpr int WIN = 4;
constexpr unsigned STG = 24 * KB;

struct WStream { rsrc_t r; unsigned v[3]; unsigned s0, step; };

struct Ring { unsigned rb[4], cur[4], slot, free; };
DI void ring_next(Ring& r) {
  r.free = r.slot;
  r.slot = r.slot == 2 * STG ? 0u : r.slot + STG;
#pragma unroll
  for (int j = 0; j < 4; ++j) { r.cur[j] = r.rb[j] + r.slot; OPQ(r.cur[j]); }
}
template <bool PI, int MODE>
DI void proj_stage(const Ctx& c, f32x4 (&acc)[12][2], u32x4 (&X)[2][2], bf16x8 (&A)[WIN], Ring& rg, int t, const WStream& ws, unsigned xs, const WStream& nx, bool has_next, unsigned l16) {
  const unsigned wdst = rg.free + c.wave * 3072;
  unsigned xo = 0;
#pragma unroll
  for (int q = 0; q < 24; ++q) {
    const int ks = q / 12, T = q % 12;
    if (q == 0) XPIN(X[0][0], X[0][1]);
    if (q == 12) {
      if (MODE == 1) { if (has_next) WAIT_V(10); else WAIT_V(7); } else if (MODE == 2) { if (has_next) WAIT_V(6); else WAIT_V(0); } else WAIT_V(10);
      XPIN(X[1][0], X[1][1]);
    }
    const bf16x8 a = A[q % WIN];
    acc[T][0] = MFMA(a, __builtin_bit_cast(bf16x8, X[ks][0]), acc[T][0]);
    acc[T][1] = MFMA(a, __builtin_bit_cast(bf16x8, X[ks][1]), acc[T][1]);
    XPIN(acc[T][0], acc[T][1]);
    SB();
    if (q == 1 || q == 4 || q == 7) {
      const int i = q / 3;
      if (MODE == 0) glds(ws.r, ws.v[i], ws.s0 + (t + 2) * ws.step, wdst + i * 1024);
      else if (has_next) glds(nx.r, nx.v[i], nx.s0 + (MODE == 2 ? nx.step : 0u), wdst + i * 1024);
      SB();
    }
    if (MODE == 0) {
      if (q == 9) { xo = faro(c, xs + (t + 2) * 4096); asm volatile("" : "+s"(xo)); SB(); }
      if (q == 12) xload<0>(X[0][0], c.ws, l16, xo);
      if (q == 14) xload<1024>(X[0][1], c.ws, l16, xo);
      if (q == 23) { xload<2048>(X[1][0], c.ws, l16, xo); xload<3072>(X[1][1], c.ws, l16, xo); }
    }
    if (q + WIN == 24 && MODE != 2) {
      if (MODE == 1) { if (has_next) WAIT_V(5); else WAIT_V(2); } else WAIT_V(7);
      LBAR();
      ring_next(rg);
      SB();
    }
    if (q + WIN < 24 || MODE != 2) {
      const int f = (q + WIN) % 24;
      A[q % WIN] = pfmt_frag<PI>(rg.cur[(f % 12) & 3] + ((f % 12) >> 2) * 128, f / 12);
      SB();
    }
  }
}
template <bool PI, int NST> DI void proj(Ctx& c, f32x4 (&acc)[12][2], const WStream& ws, unsigned xs, const WStream& nx, bool has_next, unsigned l16, int xword, unsigned xwant, bool getpar) {
  const int w = c.wave;
  u32x4 X0[2][2], X1[2][2];
  const unsigned x0 = faro(c, xs), x1 = faro(c, xs + 4096);
  stage_load(ws.r, ws.v, ws.s0, 0, w);
  stage_load(ws.r, ws.v, ws.s0 + ws.step, STG, w);
  if (xword >= 0) { if (w == 0) wait_ge(c, xword, xwant, 0x51); if (w == 1 && getpar) wait_ge(c, C_PRB, NPROBE, 0x52); BAR(); }
  const unsigned pv = getpar ? par_words(c) : 0u;
  xload<0>(X0[0][0], c.ws, l16, x0); xload<1024>(X0[0][1], c.ws, l16, x0); xload<2048>(X0[1][0], c.ws, l16, x0); xload<3072>(X0[1][1], c.ws, l16, x0);
  xload<0>(X1[0][0], c.ws, l16, x1); xload<1024>(X1[0][1], c.ws, l16, x1); xload<2048>(X1[1][0], c.ws, l16, x1); xload<3072>(X1[1][1], c.ws, l16, x1);
#pragma unroll
  for (int T = 0; T < 12; ++T) { acc[T][0] = f32x4{}; acc[T][1] = f32x4{}; XPIN(acc[T][0], acc[T][1]); }
  Ring rg;
#pragma unroll
  for (int j = 0; j < 4; ++j) rg.rb[j] = pfmt_base<PI>(c.lane, j);
  rg.slot = 2 * STG; ring_next(rg);
  SB();
  WAIT_V(0);
  XPIN(X0[0][0], X0[0][1]); XPIN(X0[1][0], X0[1][1]); XPIN(X1[0][0], X1[0][1]); XPIN(X1[1][0], X1[1][1]);
  if (getpar) par_set(c, pv);
  LBAR();
  bf16x8 A[WIN];
#pragma unroll
  for (int f = 0; f < WIN; ++f) A[f] = pfmt_frag<PI>(rg.cur[f & 3] + (f >> 2) * 128, 0);
  SB();
#pragma nounroll
  for (int t = 0; t < NST - 2; t += 2) {
    proj_stage<PI, 0>(c, acc, X0, A, rg, t, ws, xs, nx, has_next, l16);
    proj_stage<PI, 0>(c, acc, X1, A, rg, t + 1, ws, xs, nx, has_next, l16);
  }
  proj_stage<PI, 1>(c, acc, X0, A, rg, NST - 2, ws, xs, nx, has_next, l16);
  proj_stage<PI, 2>(c, acc, X1, A, rg, NST - 1, ws, xs, nx, has_next, l16);
}

struct UOut { unsigned blk, r[4]; };
DI unsigned uout_off(const Ctx& c, const UOut& u, unsigned base, int C) { return base + (u.r[C] ^ ((u.blk & 4u) << 13)); }
template <int MODE> DI void u_stage(const Ctx& c, const bf16x8 (&VF)[6][2], f32x4 (&o)[4][2], bf16x8 (&B)[WIN], Ring& rg, int t, const WStream& ws, UOut& uo, unsigned l16) {
  constexpr bool LAST = MODE == 3;
  const unsigned wdst = rg.free + c.wave * 3072;
  bf16x8 P[4];
  unsigned base = 0;
#pragma unroll
  for (int q = 0; q < 24; ++q) {
    const int m = q / 4, C = q % 4;
    const bf16x8 b = B[q % WIN];
    if (m == 0 && MODE > 0) { P[C] = pack8(o[C][0], o[C][1]); OPQ(P[C]); SB(); }
    o[C][0] = MFMA(VF[m][0], b, m == 0 ? f32x4{} : o[C][0]);
    o[C][1] = MFMA(VF[m][1], b, m == 0 ? f32x4{} : o[C][1]);
    XPIN(o[C][0], o[C][1]);
    SB();
    if (q == 2 && MODE > 0) { base = faro(c, F_UX + uo.blk * 32768u); asm volatile("" : "+s"(base)); SB(); }
    if (q % 5 == 3 && q < 20 && MODE > 0) { st16c(__builtin_bit_cast(u32x4, P[q / 5]), c.ws, l16, uout_off(c, uo, base, q / 5)); SB(); }
    if (MODE < 2 && (q == 5 || q == 10 || q == 15)) { glds(ws.r, ws.v[q / 5 - 1], ws.s0 + (t + 2) * ws.step, wdst + (q / 5 - 1) * 1024); SB(); }
    if (q + WIN == 24 && !LAST) {
      if (MODE == 0) WAIT_V(3); else if (MODE == 1) WAIT_V(7); else WAIT_V(4);
      LBAR();
      ring_next(rg);
      SB();
    }
    if (q + WIN < 24 || !LAST) { const int f = (q + WIN) % 24; B[q % WIN] = ufmt_frag(rg.cur[f % 4], f / 4); SB(); }
  }
  uo.blk += 4;
}
DI void uphase(const Ctx& c, const f32x4 (&acc)[12][2], const WStream& ws, int s, int c0, int h, unsigned l16, int nu) {
  Ring rg;
#pragma unroll
  for (int j = 0; j < 4; ++j) rg.rb[j] = ufmt_base(c.lane, j);
  rg.slot = 0; ring_next(rg);
  WAIT_V(3);
  LBAR();
  bf16x8 B[WIN], VF[6][2];
#pragma unroll
  for (int f = 0; f < WIN; ++f) B[f] = ufmt_frag(rg.cur[f % 4], f / 4);
  SB();
#pragma unroll
  for (int m = 0; m < 6; ++m) { VF[m][0] = pack8(acc[2 * m][0], acc[2 * m + 1][0]); VF[m][1] = pack8(acc[2 * m][1], acc[2 * m + 1][1]); XPIN(VF[m][0], VF[m][1]); }
  SB();
  f32x4 o[4][2];
  UOut uo; uo.blk = s * 40 + c0 - 4;
#pragma unroll
  for (int C = 0; C < 4; ++C) uo.r[C] = C * 65536 + nmap((unsigned)((h + 4 * C) & 31) * 1024u);
  u_stage<0>(c, VF, o, B, rg, 0, ws, uo, l16);
#pragma nounroll
  for (int t = 1; t < nu - 2; ++t) u_stage<1>(c, VF, o, B, rg, t, ws, uo, l16);
  u_stage<2>(c, VF, o, B, rg, nu - 2, ws, uo, l16);
  u_stage<3>(c, VF, o, B, rg, nu - 1, ws, uo, l16);
  const unsigned base = faro(c, F_UX + uo.blk * 32768u);
#pragma unroll
  for (int C = 0; C < 4; ++C) st16c(__builtin_bit_cast(u32x4, pack8(o[C][0], o[C][1])), c.ws, l16, uout_off(c, uo, base, C));
}

DI void signal_wg(const Ctx& c, int word, unsigned inc = 1) {
  WAIT_V(0);
  BAR();
  if (c.wave == 0 && c.lane == 0) __hip_atomic_fetch_add(cw(c, word), inc, RLX_AGENT);
}

DI void attn_tail(const Ctx& c, const f32x4 (&acc)[12][2], int s, int h, unsigned l16) {
  const int lane = c.lane, g = lane >> 4, tok = lane & 15;
  bf16x8 QF[6][2];
#pragma unroll
  for (int m = 0; m < 6; ++m) { QF[m][0] = pack8(acc[2 * m][0], acc[2 * m + 1][0]); QF[m][1] = pack8(acc[2 * m][1], acc[2 * m + 1][1]); }
  wait_ge(c, C_FK + (s >> 3) * 32 + h, 1, 0x600);
  f32x4 x00 = {}, x10 = {}, x11 = {}, m0 = {}, m1 = {};
  {
    u32x4 kx[12];
#pragma unroll
    for (int i = 0; i < 12; ++i) kx[i] = ld16c(c.ws, l16, nearo(c, N_KX + ((s * 32 + h) * 12 + i) * 1024));
    SB();
#pragma unroll
    for (int m = 0; m < 6; ++m) {
      x00 = MFMA(__builtin_bit_cast(bf16x8, kx[m]), QF[m][0], x00);
      x10 = MFMA(__builtin_bit_cast(bf16x8, kx[m]), QF[m][1], x10);
      x11 = MFMA(__builtin_bit_cast(bf16x8, kx[6 + m]), QF[m][1], x11);
    }
  }
  const float sc = 0.07216878364870322f * 1.4426950408889634f, NEG = -1.0e9f * 1.4426950408889634f;
  f32x4 lo[2], hi[2]; float mx[2], lx[2];
#pragma unroll
  for (int a = 0; a < 2; ++a) {
    lo[a] = a ? x10 : x00; hi[a] = x11;
#pragma unroll
    for (int i = 0; i < 4; ++i) {
      const bool keep = 4 * g + i <= tok;
      if (a == 0) { lo[a][i] = keep ? lo[a][i] * sc : NEG; hi[a][i] = NEG; } else { lo[a][i] *= sc; hi[a][i] = keep ? hi[a][i] * sc : NEG; }
    }
    mx[a] = x32max(x16max(fmaxf(fmaxf(fmaxf(lo[a][0], lo[a][1]), fmaxf(lo[a][2], lo[a][3])), fmaxf(fmaxf(hi[a][0], hi[a][1]), fmaxf(hi[a][2], hi[a][3])))));
    float l = 0.f;
#pragma unroll
    for (int i = 0; i < 4; ++i) { lo[a][i] = __builtin_amdgcn_exp2f(lo[a][i] - mx[a]); hi[a][i] = __builtin_amdgcn_exp2f(hi[a][i] - mx[a]); l += lo[a][i] + hi[a][i]; }
    lx[a] = l;
    XPIN(lo[a], hi[a]); XPIN(mx[a], lx[a]);
  }
  unsigned ko = km_soff(c, s, h), po[2][2];
#pragma unroll
  for (int a = 0; a < 2; ++a) { po[a][0] = faro(c, F_PX + (s * 2 + a) * 40960 + h * 1024); po[a][1] = faro(c, F_PX + (s * 2 + a) * 40960 + 32768 + h * 256); }
  asm volatile("" : "+s"(ko), "+s"(po[0][0]), "+s"(po[0][1]), "+s"(po[1][0]), "+s"(po[1][1]));
  const bool last = s == NS_ - 1;
  wait_ge(c, C_PS + h, last ? s : s + 1, 0x601, 1);
  {
    u32x4 km[3];
    for (unsigned spins = 0;; ++spins) {
      asm volatile("" ::: "memory");
      const bool sure = !last || ((spins & 15) == 15 && rfl(__hip_atomic_load(cw(c, C_PS + h), RLX_AGENT)) > (unsigned)s) || spins > SPIN_CAP;
#pragma unroll
      for (int i = 0; i < 3; ++i) km[i] = ld16c(c.ws, l16, ko + i * 1024);
      SB();
      if (sure) break;
      unsigned m = 0;
#pragma unroll
      for (int i = 0; i < 3; ++i) m = umax4(m, km[i]);
      if (__all(m != SENT)) break;
    }
#pragma unroll
    for (int m = 0; m < 3; ++m) {
      m0 = SMF(__builtin_bit_cast(bf16x8, km[m]), cat8(QF[2 * m][0], QF[2 * m + 1][0]), m0, (lane & 2) ? 0xEEEE : 0x4444);
      m1 = SMF(__builtin_bit_cast(bf16x8, km[m]), cat8(QF[2 * m][1], QF[2 * m + 1][1]), m1, (lane & 2) ? 0xEEEE : 0x4444);
    }
  }
#pragma unroll
  for (int a = 0; a < 2; ++a) {
    const f32x4 mm = a ? m1 : m0;
    float z0 = (mm[0] + mm[2]) * sc, z1 = (mm[1] + mm[3]) * sc;
    const float mt = fmaxf(mx[a], x32max(x16max(fmaxf(z0, z1))));
    const float f = __builtin_amdgcn_exp2f(mx[a] - mt);
    z0 = __builtin_amdgcn_exp2f(z0 - mt); z1 = __builtin_amdgcn_exp2f(z1 - mt);
    const float inv = __builtin_amdgcn_rcpf(x32sum(x16sum(lx[a] * f + (z0 + z1)))), fi = f * inv;
    st16c(__builtin_bit_cast(u32x4, pack8(lo[a] * fi, hi[a] * fi)), c.ws, l16, po[a][0]);
    st4c(pk2(z0 * inv, z1 * inv), c.ws, tok * 16 + g * 4, po[a][1]);
  }
  WAIT_V(0);
  if (lane == 0) __hip_atomic_fetch_add(cw(c, C_CP + s), 1u, RLX_AGENT);
}

DI void fin_item(const Params& p, const Ctx& c, int s, int part, unsigned l16) {
  const int lane = c.lane, w = c.wave, g = lane >> 4, tok = lane & 15;
  auto wait_words = [&](int lvl) {
    const int word = lane < 32 ? C_PV + lane : lane == 32 ? C_CP + s : lane == 33 ? C_CU + (s >> 3) : C_CG + (s >> 3);
    const unsigned full = lane > 33 ? 4u : lane == 33 ? 320u : lane == 32 ? 32u : (unsigned)s + 1u;
    const unsigned want = lane > 32 ? full : lane == 32 ? (lvl == 2 ? full : 0u) : lvl ? full : (unsigned)max(s - FIN_LEAD, 0);
    for (unsigned spins = 0;;) {
      const unsigned v = __hip_atomic_load(cw(c, word), RLX_AGENT);
      if (__all(v >= want)) return (bool)__all(v >= full);
      if (__all(lane >= 32 || v + 1 >= want)) __builtin_amdgcn_s_sleep(1); else __builtin_amdgcn_s_sleep(32);
      if (spin_fail(c, spins, 0x700)) return true;
    }
  };
  bool all = false;
  if (w == 0) all = wait_words(0);
  LBAR();
  unsigned pa = l16, pb = l16 + 40960; OPQ(pa); OPQ(pb);
  const int ct = part * 8 + w;
  u32x4 u[40];
#pragma unroll
  for (int i = 0; i < 32; ++i) u[i] = ld16c(c.ws, l16, ux_soff(c, s, ct, i & ~3) + (i & 3) * 1024);
  f32x4 gt[2]; u32x2 xv[2];
#pragma unroll
  for (int a = 0; a < 2; ++a) {
    gt[a] = __builtin_bit_cast(f32x4, ld16c(c.ws, l16, faro(c, F_G + ((s * 2 + a) * 40 + ct) * 1024)));
    xv[a] = *(const u32x2*)(p.x + (size_t)(s * 32 + a * 16 + tok) * D_ + ct * 16 + g * 4);
  }
  SB();
  if (w == 0 && !all) all = wait_words(1);
  BAR();
  const unsigned su = um_soff(c, s) + ct * 16384;
#pragma unroll
  for (int i = 0; i < 8; ++i) u[32 + i] = ld16c(c.ws, l16, su + i * 1024);
  unsigned po[5];
#pragma unroll
  for (int i = 0; i < 5; ++i) { po[i] = faro(c, F_PX + s * 81920 + (w * 10 + 2 * i) * 1024); asm volatile("" : "+s"(po[i])); }
  SB();
  const bool last = s == NS_ - 1;
  if (w == 0 && !all && !last) { wait_words(2); }
  BAR();
  {
    u32x4 pv[10];
    for (unsigned spins = 0;; ++spins) {
      asm volatile("" ::: "memory");
      const bool sure = !last || ((spins & 15) == 15 && rfl(__hip_atomic_load(cw(c, C_CP + s), RLX_AGENT)) >= 32u) || spins > SPIN_CAP;
#pragma unroll
      for (int i = 0; i < 10; ++i) pv[i] = ld16c(c.ws, l16, po[i >> 1] + (i & 1) * 1024);
      SB();
      if (sure) break;
      unsigned m = 0;
#pragma unroll
      for (int i = 0; i < 10; ++i) m = umax4(m, pv[i]);
      if (__all(m != SENT)) break;
    }
#pragma unroll
    for (int i = 0; i < 10; ++i) *(LAS u32x4*)(pa + (w * 10 + i) * 1024) = pv[i];
  }
  LBAR();
  f32x4 o[2][2] = {};
  bf16x8 b0[6], b1[6];
#pragma unroll
  for (int i = 0; i < 5; ++i) { b0[i] = ldsr16(pa + i * 1024); b1[i] = ldsr16(pb + i * 1024); }
#pragma unroll
  for (int i = 0; i < 40; ++i) {
    if (i + 5 < 40) { b0[(i + 5) % 6] = ldsr16(pa + (i + 5) * 1024); b1[(i + 5) % 6] = ldsr16(pb + (i + 5) * 1024); }
    SB();
    o[i & 1][0] = MFMA(__builtin_bit_cast(bf16x8, u[i]), b0[i % 6], o[i & 1][0]); o[i & 1][1] = MFMA(__builtin_bit_cast(bf16x8, u[i]), b1[i % 6], o[i & 1][1]);
    XPIN(o[i & 1][0], o[i & 1][1]); SB();
  }
#pragma unroll
  for (int a = 0; a < 2; ++a) {
    const f32x4 v = o[0][a] + o[1][a];
    u32x2 rr = {pk2(b2f(xv[a][0] & 0xffffu) + v[0] * gt[a][0], __uint_as_float(xv[a][0] & 0xffff0000u) + v[1] * gt[a][1]),
                pk2(b2f(xv[a][1] & 0xffffu) + v[2] * gt[a][2], __uint_as_float(xv[a][1] & 0xffff0000u) + v[3] * gt[a][3])};
    *(u32x2*)(p.out + (size_t)(s * 32 + a * 16 + tok) * D_ + ct * 16 + g * 4) = rr;
  }
}

enum { I_VU = 0, I_K = 1, I_F1 = 2, I_Q = 3, I_F2 = 4, I_FIN = 5 };
constexpr int SCHED_L = 288, SCHED_N = 64;
__device__ const unsigned SCHED[4 * SCHED_L + SCHED_N] = {
  0x50000,0x55000,0x50040,0x55040,0x50080,0x55080,0x500c0,0x550c0,0x50100,0x55100,0x50140,0x55140,0x50180,0x55180,0x501c0,0x551c0,
  0xa0001,0xa0041,0xa0081,0xa00c1,0xa0101,0xa0141,0xa0181,0xa01c1,0xa0008,0xa0048,0xa0088,0xa00c8,0xa0108,0xa0148,0xa0188,0xa01c8,
  0xa0009,0xa0049,0xa0089,0xa00c9,0xa0109,0xa0149,0xa0189,0xa01c9,0xa0010,0xa0050,0xa0090,0xa00d0,0xa0110,0xa0150,0xa0190,0xa01d0,
  0xa0004,0xa0044,0xa0084,0xa00c4,0xa0011,0xa0051,0xa0091,0xa00d1,0xa0111,0xa0151,0xa0191,0xa01d1,0xa0018,0xa0058,0xa0098,0xa0003,
  0xa00d8,0xa0118,0xa0043,0xa0158,0xa0198,0xa01d8,0xa0083,0xa0019,0xa0059,0xa00c3,0xa0099,0xa00d9,0xa0119,0xa0103,0xa0159,0xa0199,
  0xa0143,0xa01d9,0xa0024,0xa0064,0xa0183,0xa00a4,0xa00e4,0xa01c3,0xa0020,0xa0060,0xa000b,0xa00a0,0xa00e0,0xa004b,0xa0120,0xa0160,
  0xa008b,0xa01a0,0xa01e0,0xa00cb,0xa0021,0xa0061,0xa010b,0xa00a1,0xa00e1,0xa014b,0xa0121,0xa0161,0xa018b,0xa01a1,0xa01e1,0xa01cb,
  0xa0028,0xa0068,0x10005,0xa00a8,0xa0013,0xa00e8,0x11005,0xa0128,0xa0053,0x12005,0xa0168,0xa01a8,0xa0093,0x13005,0xa01e8,0x14005,
  0xa00d3,0xa0029,0xa0069,0x10105,0xa00a9,0xa0113,0xa00e9,0x11105,0xa0129,0xa0153,0x12105,0xa0169,0xa01a9,0xa0193,0x13105,0xa01e9,
  0x14105,0xa01d3,0xa0030,0xa0070,0x1000d,0xa00b0,0xa001b,0xa00f0,0x1100d,0xa0130,0xa005b,0x1200d,0xa0170,0xa01b0,0xa009b,0x1300d,
  0xa01f0,0x1400d,0xa00db,0xa0031,0xa0071,0x1010d,0xa00b1,0xa011b,0xa00f1,0x1110d,0xa0131,0xa015b,0x1210d,0xa0171,0xa01b1,0xa019b,
  0x1310d,0xa01f1,0x1410d,0xa01db,0xa0038,0xa0078,0x10015,0xa00b8,0xa0023,0xa00f8,0x11015,0xa0138,0xa0063,0x12015,0xa0178,0xa01b8,
  0xa00a3,0x13015,0xa01f8,0x14015,0xa00e3,0xa0039,0xa0079,0x10115,0xa00b9,0xa0123,0xa00f9,0x11115,0xa0139,0xa0163,0x12115,0xa0179,
  0xa01b9,0xa01a3,0x13115,0xa01f9,0x14115,0xa01e3,0x1001d,0x1101d,0xa002b,0x1201d,0xa006b,0x1301d,0xa00ab,0x1401d,0xa00eb,0x1011d,
  0x1111d,0xa012b,0x1211d,0xa016b,0x1311d,0xa01ab,0x1411d,0xa01eb,0x10025,0x11025,0x12025,0x13025,0x14025,0x10125,0x11125,0x12125,
  0x13125,0x14125,0xa0033,0xa0073,0xa00b3,0xa00f3,0xa0133,0xa0173,0xa01b3,0xa01f3,0x1002d,0x1102d,0x1202d,0x1302d,0x1402d,0x1012d,
  0x1112d,0x1212d,0x1312d,0x1412d,0xa003b,0xa007b,0xa00bb,0xa00fb,0xa013b,0xa017b,0xa01bb,0xa01fb,0x10035,0x11035,0x12035,0x13035,
  0x14035,0x10135,0x11135,0x12135,0x13135,0x14135,0x1003d,0x1103d,0x1203d,0x1303d,0x1403d,0x1013d,0x1113d,0x1213d,0x1313d,0x1413d,
  0x50200,0x55200,0x50240,0x55240,0x50280,0x55280,0x502c0,0x552c0,0x50300,0x55300,0x50340,0x55340,0x50380,0x55380,0x503c0,0x553c0,
  0xa0201,0xa0241,0xa0281,0xa02c1,0xa0301,0xa0341,0xa0381,0xa03c1,0xa0208,0xa0248,0xa0288,0xa02c8,0xa0308,0xa0348,0xa0388,0xa03c8,
  0xa0209,0xa0249,0xa0289,0xa02c9,0xa0309,0xa0349,0xa0389,0xa03c9,0xa0210,0xa0250,0xa0290,0xa02d0,0xa0310,0xa0350,0xa0390,0xa03d0,
  0xa000c,0xa004c,0xa008c,0xa00cc,0xa0211,0xa0251,0xa0291,0xa02d1,0xa0311,0xa0351,0xa0391,0xa03d1,0xa0218,0xa0258,0xa0298,0xa0203,
  0xa02d8,0xa0318,0xa0243,0xa0358,0xa0398,0xa03d8,0xa0283,0xa0219,0xa0259,0xa02c3,0xa0299,0xa02d9,0xa0319,0xa0303,0xa0359,0xa0399,
  0xa0343,0xa03d9,0xa002c,0xa006c,0xa0383,0xa00ac,0xa00ec,0xa03c3,0xa0220,0xa0260,0xa020b,0xa02a0,0xa02e0,0xa024b,0xa0320,0xa0360,
  0xa028b,0xa03a0,0xa03e0,0xa02cb,0xa0221,0xa0261,0xa030b,0xa02a1,0xa02e1,0xa034b,0xa0321,0xa0361,0xa038b,0xa03a1,0xa03e1,0xa03cb,
  0xa0228,0xa0268,0x10045,0xa02a8,0xa0213,0xa02e8,0x11045,0xa0328,0xa0253,0x12045,0xa0368,0xa03a8,0xa0293,0x13045,0xa03e8,0x14045,
  0xa02d3,0xa0229,0xa0269,0x10145,0xa02a9,0xa0313,0xa02e9,0x11145,0xa0329,0xa0353,0x12145,0xa0369,0xa03a9,0xa0393,0x13145,0xa03e9,
  0x14145,0xa03d3,0xa0230,0xa0270,0x1004d,0xa02b0,0xa021b,0xa02f0,0x1104d,0xa0330,0xa025b,0x1204d,0xa0370,0xa03b0,0xa029b,0x1304d,
  0xa03f0,0x1404d,0xa02db,0xa0231,0xa0271,0x1014d,0xa02b1,0xa031b,0xa02f1,0x1114d,0xa0331,0xa035b,0x1214d,0xa0371,0xa03b1,0xa039b,
  0x1314d,0xa03f1,0x1414d,0xa03db,0xa0238,0xa0278,0x10055,0xa02b8,0xa0223,0xa02f8,0x11055,0xa0338,0xa0263,0x12055,0xa0378,0xa03b8,
  0xa02a3,0x13055,0xa03f8,0x14055,0xa02e3,0xa0239,0xa0279,0x10155,0xa02b9,0xa0323,0xa02f9,0x11155,0xa0339,0xa0363,0x12155,0xa0379,
  0xa03b9,0xa03a3,0x13155,0xa03f9,0x14155,0xa03e3,0x1005d,0x1105d,0xa022b,0x1205d,0xa026b,0x1305d,0xa02ab,0x1405d,0xa02eb,0x1015d,
  0x1115d,0xa032b,0x1215d,0xa036b,0x1315d,0xa03ab,0x1415d,0xa03eb,0x10065,0x11065,0x12065,0x13065,0x14065,0x10165,0x11165,0x12165,
  0x13165,0x14165,0xa0233,0xa0273,0xa02b3,0xa02f3,0xa0333,0xa0373,0xa03b3,0xa03f3,0x1006d,0x1106d,0x1206d,0x1306d,0x1406d,0x1016d,
  0x1116d,0x1216d,0x1316d,0x1416d,0xa023b,0xa027b,0xa02bb,0xa02fb,0xa033b,0xa037b,0xa03bb,0xa03fb,0x10075,0x11075,0x12075,0x13075,
  0x14075,0x10175,0x11175,0x12175,0x13175,0x14175,0x1007d,0x1107d,0x1207d,0x1307d,0x1407d,0x1017d,0x1117d,0x1217d,0x1317d,0x1417d,
  0x50400,0x55400,0x50440,0x55440,0x50480,0x55480,0x504c0,0x554c0,0x50500,0x55500,0x50540,0x55540,0x50580,0x55580,0x505c0,0x555c0,
  0xa0401,0xa0441,0xa0481,0xa04c1,0xa0501,0xa0541,0xa0581,0xa05c1,0xa0408,0xa0448,0xa0488,0xa04c8,0xa0508,0xa0548,0xa0588,0xa05c8,
  0xa0409,0xa0449,0xa0489,0xa04c9,0xa0509,0xa0549,0xa0589,0xa05c9,0xa0410,0xa0450,0xa0490,0xa04d0,0xa0510,0xa0550,0xa0590,0xa05d0,
  0xa0014,0xa0054,0xa0094,0xa00d4,0xa0411,0xa0451,0xa0491,0xa04d1,0xa0511,0xa0551,0xa0591,0xa05d1,0xa0418,0xa0458,0xa0498,0xa0403,
  0xa04d8,0xa0518,0xa0443,0xa0558,0xa0598,0xa05d8,0xa0483,0xa0419,0xa0459,0xa04c3,0xa0499,0xa04d9,0xa0519,0xa0503,0xa0559,0xa0599,
  0xa0543,0xa05d9,0xa0034,0xa0074,0xa0583,0xa00b4,0xa00f4,0xa05c3,0xa0420,0xa0460,0xa040b,0xa04a0,0xa04e0,0xa044b,0xa0520,0xa0560,
  0xa048b,0xa05a0,0xa05e0,0xa04cb,0xa0421,0xa0461,0xa050b,0xa04a1,0xa04e1,0xa054b,0xa0521,0xa0561,0xa058b,0xa05a1,0xa05e1,0xa05cb,
  0xa0428,0xa0468,0x10085,0xa04a8,0xa0413,0xa04e8,0x11085,0xa0528,0xa0453,0x12085,0xa0568,0xa05a8,0xa0493,0x13085,0xa05e8,0x14085,
  0xa04d3,0xa0429,0xa0469,0x10185,0xa04a9,0xa0513,0xa04e9,0x11185,0xa0529,0xa0553,0x12185,0xa0569,0xa05a9,0xa0593,0x13185,0xa05e9,
  0x14185,0xa05d3,0xa0430,0xa0470,0x1008d,0xa04b0,0xa041b,0xa04f0,0x1108d,0xa0530,0xa045b,0x1208d,0xa0570,0xa05b0,0xa049b,0x1308d,
  0xa05f0,0x1408d,0xa04db,0xa0431,0xa0471,0x1018d,0xa04b1,0xa051b,0xa04f1,0x1118d,0xa0531,0xa055b,0x1218d,0xa0571,0xa05b1,0xa059b,
  0x1318d,0xa05f1,0x1418d,0xa05db,0xa0438,0xa0478,0x10095,0xa04b8,0xa0423,0xa04f8,0x11095,0xa0538,0xa0463,0x12095,0xa0578,0xa05b8,
  0xa04a3,0x13095,0xa05f8,0x14095,0xa04e3,0xa0439,0xa0479,0x10195,0xa04b9,0xa0523,0xa04f9,0x11195,0xa0539,0xa0563,0x12195,0xa0579,
  0xa05b9,0xa05a3,0x13195,0xa05f9,0x14195,0xa05e3,0x1009d,0x1109d,0xa042b,0x1209d,0xa046b,0x1309d,0xa04ab,0x1409d,0xa04eb,0x1019d,
  0x1119d,0xa052b,0x1219d,0xa056b,0x1319d,0xa05ab,0x1419d,0xa05eb,0x100a5,0x110a5,0x120a5,0x130a5,0x140a5,0x101a5,0x111a5,0x121a5,
  0x131a5,0x141a5,0xa0433,0xa0473,0xa04b3,0xa04f3,0xa0533,0xa0573,0xa05b3,0xa05f3,0x100ad,0x110ad,0x120ad,0x130ad,0x140ad,0x101ad,
  0x111ad,0x121ad,0x131ad,0x141ad,0xa043b,0xa047b,0xa04bb,0xa04fb,0xa053b,0xa057b,0xa05bb,0xa05fb,0x100b5,0x110b5,0x120b5,0x130b5,
  0x140b5,0x101b5,0x111b5,0x121b5,0x131b5,0x141b5,0x100bd,0x110bd,0x120bd,0x130bd,0x140bd,0x101bd,0x111bd,0x121bd,0x131bd,0x141bd,
  0x50600,0x55600,0x50640,0x55640,0x50680,0x55680,0x506c0,0x556c0,0x50700,0x55700,0x50740,0x55740,0x50780,0x55780,0x507c0,0x557c0,
  0xa0601,0xa0641,0xa0681,0xa06c1,0xa0701,0xa0741,0xa0781,0xa07c1,0xa0608,0xa0648,0xa0688,0xa06c8,0xa0708,0xa0748,0xa0788,0xa07c8,
  0xa0609,0xa0649,0xa0689,0xa06c9,0xa0709,0xa0749,0xa0789,0xa07c9,0xa0610,0xa0650,0xa0690,0xa06d0,0xa0710,0xa0750,0xa0790,0xa07d0,
  0xa001c,0xa005c,0xa009c,0xa00dc,0xa0611,0xa0651,0xa0691,0xa06d1,0xa0711,0xa0751,0xa0791,0xa07d1,0xa0618,0xa0658,0xa0698,0xa0603,
  0xa06d8,0xa0718,0xa0643,0xa0758,0xa0798,0xa07d8,0xa0683,0xa0619,0xa0659,0xa06c3,0xa0699,0xa06d9,0xa0719,0xa0703,0xa0759,0xa0799,
  0xa0743,0xa07d9,0xa003c,0xa007c,0xa0783,0xa00bc,0xa00fc,0xa07c3,0xa0620,0xa0660,0xa060b,0xa06a0,0xa06e0,0xa064b,0xa0720,0xa0760,
  0xa068b,0xa07a0,0xa07e0,0xa06cb,0xa0621,0xa0661,0xa070b,0xa06a1,0xa06e1,0xa074b,0xa0721,0xa0761,0xa078b,0xa07a1,0xa07e1,0xa07cb,
  0xa0628,0xa0668,0x100c5,0xa06a8,0xa0613,0xa06e8,0x110c5,0xa0728,0xa0653,0x120c5,0xa0768,0xa07a8,0xa0693,0x130c5,0xa07e8,0x140c5,
  0xa06d3,0xa0629,0xa0669,0x101c5,0xa06a9,0xa0713,0xa06e9,0x111c5,0xa0729,0xa0753,0x121c5,0xa0769,0xa07a9,0xa0793,0x131c5,0xa07e9,
  0x141c5,0xa07d3,0xa0630,0xa0670,0x100cd,0xa06b0,0xa061b,0xa06f0,0x110cd,0xa0730,0xa065b,0x120cd,0xa0770,0xa07b0,0xa069b,0x130cd,
  0xa07f0,0x140cd,0xa06db,0xa0631,0xa0671,0x101cd,0xa06b1,0xa071b,0xa06f1,0x111cd,0xa0731,0xa075b,0x121cd,0xa0771,0xa07b1,0xa079b,
  0x131cd,0xa07f1,0x141cd,0xa07db,0xa0638,0xa0678,0x100d5,0xa06b8,0xa0623,0xa06f8,0x110d5,0xa0738,0xa0663,0x120d5,0xa0778,0xa07b8,
  0xa06a3,0x130d5,0xa07f8,0x140d5,0xa06e3,0xa0639,0xa0679,0x101d5,0xa06b9,0xa0723,0xa06f9,0x111d5,0xa0739,0xa0763,0x121d5,0xa0779,
  0xa07b9,0xa07a3,0x131d5,0xa07f9,0x141d5,0xa07e3,0x100dd,0x110dd,0xa062b,0x120dd,0xa066b,0x130dd,0xa06ab,0x140dd,0xa06eb,0x101dd,
  0x111dd,0xa072b,0x121dd,0xa076b,0x131dd,0xa07ab,0x141dd,0xa07eb,0x100e5,0x110e5,0x120e5,0x130e5,0x140e5,0x101e5,0x111e5,0x121e5,
  0x131e5,0x141e5,0xa0633,0xa0673,0xa06b3,0xa06f3,0xa0733,0xa0773,0xa07b3,0xa07f3,0x100ed,0x110ed,0x120ed,0x130ed,0x140ed,0x101ed,
  0x111ed,0x121ed,0x131ed,0x141ed,0xa063b,0xa067b,0xa06bb,0xa06fb,0xa073b,0xa077b,0xa07bb,0xa07fb,0x100f5,0x110f5,0x120f5,0x130f5,
  0x140f5,0x101f5,0x111f5,0x121f5,0x131f5,0x141f5,0x100fd,0x110fd,0x120fd,0x130fd,0x140fd,0x101fd,0x111fd,0x121fd,0x131fd,0x141fd,
  0xa0002,0xa0042,0xa0082,0xa00c2,0xa0102,0xa0142,0xa0182,0xa01c2,0xa000a,0xa004a,0xa008a,0xa00ca,0xa010a,0xa014a,0xa018a,0xa01ca,
  0xa0012,0xa0052,0xa0092,0xa00d2,0xa0112,0xa0152,0xa0192,0xa01d2,0xa001a,0xa005a,0xa009a,0xa00da,0xa011a,0xa015a,0xa019a,0xa01da,
  0xa0022,0xa0062,0xa00a2,0xa00e2,0xa0122,0xa0162,0xa01a2,0xa01e2,0xa002a,0xa006a,0xa00aa,0xa00ea,0xa012a,0xa016a,0xa01aa,0xa01ea,
  0xa0032,0xa0072,0xa00b2,0xa00f2,0xa0132,0xa0172,0xa01b2,0xa01f2,0xa003a,0xa007a,0xa00ba,0xa00fa,0xa013a,0xa017a,0xa01ba,0xa01fa,
};
DI rsrc_t mk_rsrc_u(const void* p, unsigned bytes) {
  const unsigned long long a = (unsigned long long)p;
  return mk_rsrc((const void*)(((unsigned long long)rfl((unsigned)(a >> 32)) << 32) | rfl((unsigned)a)), rfl(bytes));
}
DI void bulk(const Params& p, Ctx c0, int q, int first, bool once) {
  volatile LAS unsigned* tk = (volatile LAS unsigned*)(LDS_BYTES - 16);
  int word = -1; unsigned inc = 1;
  bool havepar = false; unsigned xok = 0;
  for (int round = 0;; ++round) {
    Ctx c = c0;
    asm volatile("v_mbcnt_lo_u32_b32 %0, -1, 0\n\tv_mbcnt_hi_u32_b32 %0, -1, %0" : "=v"(c.lane));
    asm volatile("" : "+s"(c.wave));
    const int lane = c.lane, w = c.wave, g = lane >> 4;
    unsigned l16 = (lane * 16) & 0x3F0u;
    int it = first;
    if (round) {
      if (once) { signal_wg(c, word, inc); break; }
      WAIT_V(0);
      LBAR();
      if (w == 0 && lane == 0) {
        if (word >= 0) __hip_atomic_fetch_add(cw(c, word), inc, RLX_AGENT);
        *tk = __hip_atomic_fetch_add(cw(c, C_Q + q), 1u, RLX_AGENT);
      }
      LBAR();
      it = (int)rfl(*tk);
    }
    word = -1; inc = 1;
    if (it >= SCHED_L) break;
    const unsigned code = rfl(SCHED[q * SCHED_L + it]);
    const int type = code & 7, rb = (code >> 3) & 7, h = (code >> 6) & 63, u0 = (code >> 12) & 15, nu = code >> 16;
    const int s = rb * 8 + w;
    if (!havepar && type > I_Q) { load_par(c, C_PRB, NPROBE); c0.par = c.par; havepar = true; }
    if (type == I_FIN) { fin_item(p, c, rb * 8 + h, u0, l16); if (it >= SCHED_L - 10) break; continue; }
    f32x4 acc[12][2];
    if (type == I_F2) {
      const int n0 = h < 3 ? h * 192 : 448;
      WStream ws; ws.r = mk_rsrc(p.w2, F_ * D_ * 2); pfmt_voff<true>(ws.v, w, lane, 1280); ws.s0 = n0 * 2; ws.step = 64 * 1280;
      proj<true, 24>(c, acc, ws, F_HF + s * 2 * 48 * 1024, ws, false, l16, C_F1 + rb, 8, false);
#pragma unroll
      for (int T = 0; T < 12; ++T) {
        const u32x2 bv = *(const u32x2*)(p.b2 + n0 + T * 16 + g * 4);
        const f32x4 bb = {b2f(bv[0] & 0xffffu), __uint_as_float(bv[0] & 0xffff0000u), b2f(bv[1] & 0xffffu), __uint_as_float(bv[1] & 0xffff0000u)};
#pragma unroll
        for (int a = 0; a < 2; ++a) {
          f32x4 v = acc[T][a] + bb;
#pragma unroll
          for (int i = 0; i < 4; ++i) v[i] = v[i] / (1.0f + __expf(-v[i]));
          if (h < 3 || T >= 8) st16c(__builtin_bit_cast(u32x4, v), c.ws, l16, faro(c, F_G + ((s * 2 + a) * 40 + n0 / 16 + T) * 1024));
        }
      }
      word = C_CG + rb;
      continue;
    }
    WStream ws, wo;
    const unsigned pitch = type == I_F1 ? 3072 : 12288;
    ws.r = mk_rsrc_u(type == I_VU ? p.Wv : type == I_K ? p.Wk : type == I_F1 ? p.w1 : p.Wq, D_ * pitch);
    pfmt_voff<false>(ws.v, w, lane, pitch); ws.s0 = h * 384; ws.step = rfl(64 * pitch);
    wo.r = mk_rsrc(p.Wo, D_ * H_ * E_ * 2); ufmt_voff(wo.v, w, lane); wo.s0 = h * 192 * 1280 + u0 * 128; wo.step = 128;
    proj<false, 10>(c, acc, ws, F_XN + s * 2 * 20 * 1024, wo, type == I_VU, l16, (xok >> rb) & 1 ? -1 : C_X + rb, 16, !havepar);
    xok |= 1u << rb; c0.par = c.par; havepar = true;
    if (type == I_VU) {
      uphase(c, acc, wo, s, u0 * 4, h, l16, nu);
      word = C_CU + rb; inc = nu;
    } else if (type == I_K) {
#pragma unroll
      for (int a = 0; a < 2; ++a)
#pragma unroll
        for (int m = 0; m < 6; ++m) st16c(__builtin_bit_cast(u32x4, pack8(acc[2 * m][a], acc[2 * m + 1][a])), c.ws, l16, nearo(c, N_KX + ((s * 32 + h) * 12 + a * 6 + m) * 1024));
      word = C_FK + rb * 32 + h;
    } else if (type == I_F1) {
#pragma unroll
      for (int T = 0; T < 12; ++T) {
        const u32x2 bv = *(const u32x2*)(p.b1 + h * 192 + T * 16 + g * 4);
        const f32x4 bb = {b2f(bv[0] & 0xffffu), __uint_as_float(bv[0] & 0xffff0000u), b2f(bv[1] & 0xffffu), __uint_as_float(bv[1] & 0xffff0000u)};
#pragma unroll
        for (int a = 0; a < 2; ++a) {
          f32x4 v = acc[T][a] + bb;
#pragma unroll
          for (int i = 0; i < 4; ++i) { const float r = v[i] > 0.f ? v[i] : 0.f; v[i] = r * r; }
          acc[T][a] = v;
        }
      }
#pragma unroll
      for (int a = 0; a < 2; ++a)
#pragma unroll
        for (int m = 0; m < 6; ++m) st16c(__builtin_bit_cast(u32x4, pack8(acc[2 * m][a], acc[2 * m + 1][a])), c.ws, l16, faro(c, F_HF + ((s * 48 + h * 6 + m) * 2 + a) * 1024));
      word = C_F1 + rb;
    } else {
      attn_tail(c, acc, s, h, l16);
    }
  }
}

DI void ln_tile(const Params& p, const Ctx& c, int b) {
  const int lane = c.lane, w = c.wave, tok = lane & 15, g = lane >> 4;
  volatile LAS float* red = (volatile LAS float*)(4096);
  const u32x4* src = (const u32x4*)(p.x + (size_t)(b * 16 + tok) * D_ + w * 160 + g * 8);
  float v[40];
#pragma unroll
  for (int k = 0; k < 5; ++k) { u32x4 d = src[k * 4];
#pragma unroll
    for (int j = 0; j < 4; ++j) { v[k * 8 + 2 * j] = b2f(d[j] & 0xffffu); v[k * 8 + 2 * j + 1] = __uint_as_float(d[j] & 0xffff0000u); } }
  float s = 0.f, q = 0.f;
#pragma unroll
  for (int i = 0; i < 40; ++i) s += v[i];
  const float m = x32sum(x16sum(s)) * (1.0f / 160);
#pragma unroll
  for (int i = 0; i < 40; ++i) q += (v[i] - m) * (v[i] - m);
  red[w * 16 + tok] = m; red[64 + w * 16 + tok] = x32sum(x16sum(q));
  *(volatile LAS unsigned char*)(4096 + 512 + w) = 1;
  for (int sp = 1 << 22; --sp;) { if (rfl(*(volatile LAS unsigned*)(4096 + 512)) == 0x01010101u) break; }
  const float m0 = red[tok], m1 = red[16 + tok], m2 = red[32 + tok], m3 = red[48 + tok], mean = 0.25f * ((m0 + m1) + (m2 + m3));
  const float var = ((red[64 + tok] + red[80 + tok]) + (red[96 + tok] + red[112 + tok])) * (1.0f / D_)
                  + 0.25f * (((m0 - mean) * (m0 - mean) + (m1 - mean) * (m1 - mean)) + ((m2 - mean) * (m2 - mean) + (m3 - mean) * (m3 - mean)));
  const float rs = 1.0f / sqrtf(var + 1e-5f);
#pragma unroll
  for (int k = 0; k < 5; ++k) { u32x4 d;
#pragma unroll
    for (int j = 0; j < 4; ++j) d[j] = pk2((v[k * 8 + 2 * j] - mean) * rs, (v[k * 8 + 2 * j + 1] - mean) * rs);
    const unsigned o = nmap(F_XN + (((b >> 1) * 20 + w * 5 + k) * 2 + (b & 1)) * 1024);
    st16c(d, c.ws, lane * 16, o); st16c(d, c.ws, lane * 16, o + 8192); }
}
DI void setup_part(const Params& p, Ctx c, int j) {
  const int lane = c.lane, w = c.wave, tid = w * 64 + lane;
  load_par(c, C_PRB, NPROBE);
  if (j < 5) {
    const int i = j * 512 + tid, r = i / 320, cp = i % 320, ct = cp >> 3, q = cp & 7;
    unsigned v = ((const unsigned*)p.mem0)[i]; v = v < 0xFFFFFFFEu ? v : 0xFFFFFFFEu;
    st4c(v, c.ws, nearo(c, ct * 256 + (4 * (r >> 1) + 2 * (q & 1) + (r & 1)) * 16 + (q >> 1) * 4), 0);
  }
  const u32x4 sv = {SENT, SENT, SENT, SENT};
  constexpr unsigned n0 = MEM_BYTES / 16, n1 = (2 * MB + 640 * KB) / 16, n2 = n1 + 64 * KB / 16;
  for (unsigned i = n0 + j * 512 + tid; i < n2; i += 24 * 512) st16c(sv, c.ws, nearo(c, i < n1 ? i * 16 : N_TP + (i - n1) * 16), 0);
  for (unsigned i = j * 512 + tid; i < (128 + 80) * 64; i += 24 * 512) st16c(sv, c.ws, i < 128 * 64 ? nearo(c, N_KM + 63 * 32 * 4096 + i * 16) : faro(c, F_PX + 63 * 81920 + (i - 128 * 64) * 16), 0);
  signal_wg(c, C_RDY);
}

DI int ctx_init(Ctx& c, const Params& p) {
  char* base = ws_base(p.ws);
  c.ws = mk_rsrc(base, WS_END);
  c.ctl = (gu32*)(base + WS_CTL);
  c.lane = threadIdx.x & 63; c.wave = rfl(threadIdx.x >> 6);
  c.par = 0; c.t0 = 0;
  const int b = blockIdx.x, tid = threadIdx.x;
  gu32* hb = cw(c, C_HB + (b == 0 ? (tid & 7) : (b & 7)));
  unsigned h0 = 0;
  if (tid == 0) *(volatile LAS unsigned*)(4096 + 512) = 0;
  LBAR();
  if (c.wave == 4) h0 = __hip_atomic_load(hb, RLX_AGENT);
  if (c.wave == 6 && b < 16)
    asm volatile("s_mov_b32 m0, %0\n\ts_nop 4\n\tbuffer_load_dword %1, %2, 0 offen lds" ::"s"(16640), "v"(c.lane * 128), "s"(mk_rsrc_u(SCHED, sizeof(SCHED))) : "memory");
  if (c.wave == 7) {
    unsigned long long pc; int left;
    asm volatile("s_getpc_b64 %0\n.Lce:\n\ts_mov_b32 %1, 0\n\ts_max_i32 %1, %1, .Lce0-.Lce\n\ts_max_i32 %1, %1, .Lce1-.Lce\n\ts_max_i32 %1, %1, .Lce2-.Lce\n\ts_max_i32 %1, %1, .Lce3-.Lce\n\t"
                 "s_max_i32 %1, %1, .Lce4-.Lce\n\ts_max_i32 %1, %1, .Lce5-.Lce\n\ts_max_i32 %1, %1, .Lce6-.Lce" : "=s"(pc), "=s"(left) :: "scc");
    asm volatile("s_mov_b32 m0, %0\n\ts_nop 4\n\tbuffer_load_dword %1, %2, 0 offen lds" ::"s"(16384), "v"(((b >> 3) * 32 + (c.lane & 31)) * 128), "s"(mk_rsrc((const void*)pc, (unsigned)left)) : "memory");
  }
  if (b == 0) {
    if (tid < C_N) __hip_atomic_store(cw(c, tid), tid == C_T0 ? (unsigned)__builtin_amdgcn_s_memrealtime() : tid >= C_Q && tid < C_Q + 4 ? 32u : 0u, RLX_AGENT);
    WAIT_V(0);
    BAR();
    if (c.wave == 4) {
      for (unsigned spins = 0; spins < SPIN_CAP; ++spins) {
        if (c.lane < 8) __hip_atomic_store(hb, ++h0, RLX_AGENT);
        const unsigned n = min(__hip_atomic_load(cw(c, C_TK + (c.lane & 7)), RLX_AGENT), 32u);
        unsigned sum = 0;
#pragma unroll
        for (int i = 0; i < 8; ++i) sum += __builtin_amdgcn_readlane(n, i);
        if (sum >= 255u) break;
      }
    }
  } else {
    if (b & 1) { if (c.wave < 4) ln_tile(p, c, b >> 1); }
    else if (b <= 2 * NPROBE && c.wave == 0) {
      const unsigned o = (b / 2 - 1) * (8 * MB) + 7 * MB;
      unsigned best[2] = {~0u, ~0u};
      for (int rep = 0; rep < 3; ++rep)
#pragma unroll
        for (int hh = 0; hh < 2; ++hh) {
          const unsigned long long t0 = __builtin_amdgcn_s_memtime();
          st4c(SENT, c.ws, (c.lane & 31) * 4, o + hh * 8192); WAIT_V(0);
          best[hh] = min(best[hh], (unsigned)(__builtin_amdgcn_s_memtime() - t0));
        }
      if (c.lane == 0) __hip_atomic_store(cw(c, C_PAR + b / 2 - 1), (unsigned)(best[1] < best[0]) ^ (unsigned)(xcc_id() >= 4) ^ 1u, RLX_AGENT);
    }
    if (c.wave == 4) {
      for (unsigned spins = 0; spins < SPIN_CAP && rfl(__hip_atomic_load(hb, RLX_AGENT)) == rfl(h0); ++spins) __builtin_amdgcn_s_sleep(1);
    }
  }
  LAS unsigned* tk = (LAS unsigned*)(LDS_BYTES - 16);
  if (tid == 256) {
    unsigned gq = xcc_id() & 7, r = 0;
    for (int k = 0; k < 8; ++k, gq = (gq + 1) & 7) {
      const unsigned t = __hip_atomic_fetch_add(cw(c, C_TK + gq), 1u, RLX_AGENT);
      if (t < 32u) { r = gq < 4 ? 4 * t + gq : 32 * gq + t; break; }
    }
    *tk = r;
  }
  WAIT_V(0);
  LBAR();
  if (tid == 0 && ((b & 1) || (b && b <= 2 * NPROBE))) __hip_atomic_fetch_add(cw(c, (b & 1) ? C_X + (b >> 5) : C_PRB), 1u, RLX_AGENT);
  const int r = (int)rfl(*tk);
  LBAR();
  return r;
}

__global__ void __launch_bounds__(512, 2) k_main(Params p) {
  extern __shared__ __attribute__((aligned(16))) char smem[];
  Ctx c;
  const int role = ctx_init(c, p);
#define ROLE(n, call) do { asm volatile("v_mbcnt_lo_u32_b32 %0, -1, 0\n\tv_mbcnt_hi_u32_b32 %0, -1, %0" : "=v"(c.lane)); call; asm volatile(".Lce" #n ":"); __builtin_amdgcn_endpgm(); } while (0)
  if (role < 32) { if (c.wave < 2) ROLE(0, s_wave<0>(p, c, role)); else if (c.wave == 2) ROLE(1, s_wave<1>(p, c, role)); else if (c.wave == 3) ROLE(2, s_wave<3>(p, c, role)); else ROLE(3, s_wave<2>(p, c, role)); }
  else if (role < 64) { if (c.wave < 4) ROLE(4, v_wave<true>(p, c, role - 32)); else ROLE(5, v_wave<false>(p, c, role - 32)); }
  asm volatile("v_mbcnt_lo_u32_b32 %0, -1, 0\n\tv_mbcnt_hi_u32_b32 %0, -1, %0" : "=v"(c.lane));
  if (role >= NCHAIN && role < 128) setup_part(p, c, role - NCHAIN);
  bulk(p, c, role < 128 ? 4 : (role - 128) >> 5, role < 128 ? role - 64 : role & 31, role < 128);
  if (role < NCHAIN) ROLE(6, m_wave(p, c, role - 64));
}

extern "C" void kernel_launch(void* const* d_in, const int* in_sizes, int n_in, void* d_out, int out_size, void* d_ws, size_t ws_size, hipStream_t stream) {
  Params p;
  p.x = (const u16*)d_in[0]; p.mem0 = (const u16*)d_in[1]; p.Wq = (const u16*)d_in[2]; p.Wk = (const u16*)d_in[3]; p.Wv = (const u16*)d_in[4]; p.Wo = (const u16*)d_in[5];
  p.w1 = (const u16*)d_in[8]; p.b1 = (const u16*)d_in[9]; p.w2 = (const u16*)d_in[10]; p.b2 = (const u16*)d_in[11];
  p.out = (u16*)d_out; p.ws = (char*)d_ws;
  hipFuncSetAttribute((const void*)k_main, hipFuncAttributeMaxDynamicSharedMemorySize, LDS_BYTES);
  k_main<<<256, 512, LDS_BYTES, stream>>>(p);
}
```
